```python
import torch

def _softmax(x, axis=-1):
    return torch.softmax(x, dim=axis)

def _gelu(x):
    return torch.nn.functional.gelu(x, approximate='tanh')

def _silu(x):
    return torch.nn.functional.silu(x)

def _sigmoid(x):
    return torch.sigmoid(x)

def _layernorm(x, axis=-1, eps=1e-05):
    m = x.mean(dim=axis, keepdim=True)
    v = x.var(dim=axis, keepdim=True, unbiased=False)
    return (x - m) / torch.sqrt(v + eps)

def _scatter_add(tgt, idx, src, axis=0):
    return tgt.clone().index_add_(axis, idx, src)

def _segment_sum(src, seg, n):
    out = torch.zeros((n,) + src.shape[1:], dtype=src.dtype)
    return out.index_add_(0, seg, src)

def _pad(x, pw):
    flat = tuple((v for pair in reversed(pw) for v in pair))
    return torch.nn.functional.pad(x, flat)
kernel_name = 'recurrent_scan__memory_aug_recurrent_mem_tr__dense_activation_mish__prompt'

def setup_inputs():

    _w = lambda a: (lambda t: t.to(torch.bfloat16) if t.is_floating_point() else t)(torch.as_tensor(a))

    def _A_setup():
        B, S, Dm = (1, 2048, 640)
        H, Dh = (32, 128)
        M, seg = (8, 32)
        seg = min(seg, S)
        S = S // seg * seg
        return {'x': _w((lambda *s: torch.randn(*s, dtype=torch.bfloat16) * 0.05)(B, S, Dm)), 'mem0': _w((lambda *s: torch.randn(*s, dtype=torch.bfloat16) * 0.8)(B, M, Dm)), 'Wq': _w((lambda *s: torch.randn(*s, dtype=torch.bfloat16) * 0.8)(Dm, H, Dh) * Dm ** (-0.5)), 'Wk': _w((lambda *s: torch.randn(*s, dtype=torch.bfloat16) * 0.8)(Dm, H, Dh) * Dm ** (-0.5)), 'Wv': _w((lambda *s: torch.randn(*s, dtype=torch.bfloat16) * 0.8)(Dm, H, Dh) * Dm ** (-0.5)), 'Wo': _w((lambda *s: torch.randn(*s, dtype=torch.bfloat16) * 0.8)(H, Dh, Dm) * (H * Dh) ** (-0.5)), 'seg_len': seg}
    _A_in = _A_setup()

    def _B_setup():
        B, S, D, F = (1, 2048, 640, 1728)
        return {'x': _w((lambda *s: torch.randn(*s, dtype=torch.bfloat16) * 0.05)(B, S, D)), 'w1': _w((lambda *s: torch.randn(*s, dtype=torch.bfloat16) * 0.8)(D, F) * D ** (-0.5)), 'b1': _w((lambda *s: torch.randn(*s, dtype=torch.bfloat16) * 0.4)(F)), 'w2': _w((lambda *s: torch.randn(*s, dtype=torch.bfloat16) * 0.2)(F, D) * F ** (-0.5))}
    _B_in = _B_setup()
    return {'A_x': _A_in['x'], 'A_mem0': _A_in['mem0'], 'A_Wq': _A_in['Wq'], 'A_Wk': _A_in['Wk'], 'A_Wv': _A_in['Wv'], 'A_Wo': _A_in['Wo'], 'A_seg_len': _A_in['seg_len'], 'B_x': _B_in['x'], 'B_w1': _B_in['w1'], 'B_b1': _B_in['b1'], 'B_w2': _B_in['w2']}

def reference(A_x, A_mem0, A_Wq, A_Wk, A_Wv, A_Wo, A_seg_len, B_x, B_w1, B_b1, B_w2):

    def _A_ref(x, mem0, Wq, Wk, Wv, Wo, seg_len):
        B, S, Dm = x.shape
        H, Dh = (Wq.shape[1], Wq.shape[2])
        M = mem0.shape[1]
        scale = Dh ** (-0.5)
        n_seg = S // seg_len
        L = M + seg_len
        idx = torch.arange(L)
        mask = (idx[:, None] < M) | (idx[:, None] >= idx[None, :])
        mem = mem0
        outs = []
        for s in range(n_seg):
            xs = x[:, s * seg_len:(s + 1) * seg_len, :].clone()
            h = torch.concatenate([mem, xs], axis=1)
            q = torch.einsum('btd,dhe->bhte', h, Wq)
            k = torch.einsum('btd,dhe->bhte', h, Wk)
            v = torch.einsum('btd,dhe->bhte', h, Wv)
            scores = torch.einsum('bhqe,bhke->bhqk', q, k) * scale
            scores = torch.where(mask[None, None], scores, -1000000000.0)
            p = _softmax(scores, axis=-1)
            o = torch.einsum('bhqk,bhke->bhqe', p, v)
            o = torch.einsum('bhte,hed->btd', o, Wo)
            mem = o[:, :M, :].clone()
            outs.append(o[:, M:, :])
        return torch.concatenate(outs, axis=1)

    def _B_ref(x, w1, b1, w2):
        h = torch.einsum('bsd,df->bsf', x, w1) + b1
        sp = torch.log1p(torch.exp(-torch.abs(h))) + torch.where(h > 0, h, torch.zeros_like(h))
        act = h * torch.tanh(sp)
        return torch.einsum('bsf,fd->bsd', act, w2)
    x = A_x
    xn = _layernorm(x, axis=-1)
    s = _A_ref(xn, A_mem0, A_Wq, A_Wk, A_Wv, A_Wo, A_seg_len)
    g = _silu(_B_ref(xn, B_w1, B_b1, B_w2))
    return x + s * g
```

```cpp
#include <hip/hip_runtime.h>
#include <hip/hip_bf16.h>
#include <stdint.h>

#define WORKER_ASM \
  "s_mov_b32 s36, %[wave]\nv_mbcnt_lo_u32_b32 v105, -1, 0\nv_mbcnt_hi_u32_b32 v105, -1, v105\ns_lshl_b32 s62, s36, 8\nv_lshlrev_b32 v105, 2, v105\nv_add_u32 v105, s62, v105\n" \
  "ds_read_b32 v0, v105 offset:0\nds_read_b32 v1, v105 offset:1024\nds_read_b32 v4, v105 offset:4096\nds_read_b32 v5, v105 offset:5120\nds_read_b32 v6, v105 offset:6144\n" \
  "ds_read_b32 v7, v105 offset:7168\nds_read_b32 v8, v105 offset:8192\nds_read_b32 v9, v105 offset:9216\nds_read_b32 v10, v105 offset:10240\nds_read_b32 v11, v105 offset:11264\n" \
  "ds_read_b32 v107, v105 offset:24576\ns_waitcnt lgkmcnt(0)\nds_read_b32 v96, v105 offset:12288\nds_read_b32 v97, v105 offset:13312\nds_read_b32 v98, v105 offset:14336\n" \
  "ds_read_b32 v99, v105 offset:15360\nds_read_b32 v100, v105 offset:16384\nds_read_b32 v101, v105 offset:17408\nds_read_b32 v102, v105 offset:18432\nds_read_b32 v103, v105 offset:19456\n" \
  "ds_read_b32 v104, v105 offset:20480\nv_mov_b32 v12, 0x3f803f80\nv_mov_b32 v13, 0x3f803f80\nv_mov_b32 v14, 0x3f803f80\nv_mov_b32 v15, 0x3f803f80\nv_readlane_b32 s20, v107, 0\n" \
  "v_readlane_b32 s21, v107, 1\nv_readlane_b32 s22, v107, 2\nv_readlane_b32 s23, v107, 3\nv_readlane_b32 s24, v107, 4\nv_readlane_b32 s25, v107, 5\nv_readlane_b32 s26, v107, 6\n" \
  "v_readlane_b32 s27, v107, 7\nv_readlane_b32 s28, v107, 8\nv_readlane_b32 s29, v107, 9\nv_readlane_b32 s30, v107, 10\nv_readlane_b32 s31, v107, 11\nv_readlane_b32 s18, v107, 12\n" \
  "v_readlane_b32 s19, v107, 13\nv_readlane_b32 s34, v107, 14\nv_readlane_b32 s14, v107, 15\nv_readlane_b32 s38, v107, 16\nv_readlane_b32 s39, v107, 17\nv_readlane_b32 s90, v107, 18\n" \
  "v_readlane_b32 s91, v107, 19\nv_readlane_b32 s92, v107, 20\nv_readlane_b32 s93, v107, 21\nv_readlane_b32 s40, v107, 22\nv_readlane_b32 s41, v107, 23\nv_readlane_b32 s68, v107, 24\n" \
  "v_readlane_b32 s69, v107, 25\nv_readlane_b32 s44, v107, 26\nv_readlane_b32 s45, v107, 27\nv_readlane_b32 s48, v107, 28\nv_readlane_b32 s49, v107, 29\nv_readlane_b32 s50, v107, 30\n" \
  "v_readlane_b32 s51, v107, 31\nv_readlane_b32 s52, v107, 32\nv_readlane_b32 s53, v107, 33\nv_readlane_b32 s54, v107, 34\nv_readlane_b32 s55, v107, 35\nv_readlane_b32 s60, v107, 36\n" \
  "v_readlane_b32 s15, v107, 37\nv_readlane_b32 s12, v107, 38\nv_readlane_b32 s13, v107, 39\nv_readlane_b32 s10, v107, 40\nv_readlane_b32 s11, v107, 41\nv_readlane_b32 s94, v107, 42\n" \
  "v_readlane_b32 s95, v107, 43\nv_readlane_b32 s70, v107, 44\nv_readlane_b32 s71, v107, 45\nv_readlane_b32 s72, v107, 46\nv_readlane_b32 s73, v107, 47\nv_readlane_b32 s74, v107, 48\n" \
  "v_readlane_b32 s75, v107, 49\nv_readlane_b32 s42, v107, 50\nv_readlane_b32 s43, v107, 51\ns_nop 4\ns_add_u32 s46, s44, 0x20000\ns_addc_u32 s47, s45, 0\n" \
  "s_mov_b32 s56, 0\ns_mov_b32 s57, 1\ns_mov_b32 s59, 0x3e0293ee\ns_mov_b32 s58, 0x200000\ns_lshl_b32 s87, s36, 11\ns_add_u32 s87, s87, 139264\ns_add_u32 s89, s87, 12288\n" \
  "s_xor_b32 s89, s89, s87\ns_lshl_b32 s88, s36, 10\ns_add_u32 s88, s88, 147456\ns_add_u32 s61, s88, 12288\ns_xor_b32 s61, s61, s88\ns_waitcnt vmcnt(20) lgkmcnt(0)\n" \
  "s_barrier\nv_readfirstlane_b32 s62, v108\nv_readfirstlane_b32 s63, v109\ns_nop 3\ns_add_u32 s62, s62, 32\ns_addc_u32 s73, s63, 0\ns_andn2_b32 s62, s62, 31\n" \
  "s_or_b32 s72, s72, s62\ns_cmp_eq_u32 s36, 3\ns_cbranch_scc0 64f\nv_mov_b32 v108, s72\nv_mov_b32 v109, s73\nv_mov_b64 v[110:111], 0\ns_mov_b64 exec, 0xff\n" \
  "global_store_dwordx4 v1, v[108:111], s[74:75] sc1\ns_mov_b64 exec, -1\ns_nop 1\n64:\ns_mov_b32 s67, 524288\ns_add_u32 s62, s20, s67\ns_addc_u32 s63, s21, 0\n" \
  "s_lshl_b32 s66, s36, 12\ns_add_u32 s66, s66, 0\ns_mov_b32 m0, s66\ns_add_u32 s66, s66, 1024\nglobal_load_lds_dwordx4 v96, s[62:63]\ns_add_u32 s62, s62, 32768\n" \
  "s_addc_u32 s63, s63, 0\ns_mov_b32 m0, s66\ns_add_u32 s66, s66, 1024\nglobal_load_lds_dwordx4 v96, s[62:63]\ns_add_u32 s62, s62, 32768\ns_addc_u32 s63, s63, 0\n" \
  "s_mov_b32 m0, s66\ns_add_u32 s66, s66, 1024\nglobal_load_lds_dwordx4 v96, s[62:63]\ns_add_u32 s62, s62, 32768\ns_addc_u32 s63, s63, 0\ns_mov_b32 m0, s66\n" \
  "s_add_u32 s66, s66, 1024\nglobal_load_lds_dwordx4 v96, s[62:63]\ns_add_u32 s62, s22, s67\ns_addc_u32 s63, s23, 0\ns_lshl_b32 s66, s36, 12\ns_add_u32 s66, s66, 16384\n" \
  "s_mov_b32 m0, s66\ns_add_u32 s66, s66, 1024\nglobal_load_lds_dwordx4 v96, s[62:63]\ns_add_u32 s62, s62, 32768\ns_addc_u32 s63, s63, 0\ns_mov_b32 m0, s66\n" \
  "s_add_u32 s66, s66, 1024\nglobal_load_lds_dwordx4 v96, s[62:63]\ns_add_u32 s62, s62, 32768\ns_addc_u32 s63, s63, 0\ns_mov_b32 m0, s66\ns_add_u32 s66, s66, 1024\n" \
  "global_load_lds_dwordx4 v96, s[62:63]\ns_add_u32 s62, s62, 32768\ns_addc_u32 s63, s63, 0\ns_mov_b32 m0, s66\ns_add_u32 s66, s66, 1024\nglobal_load_lds_dwordx4 v96, s[62:63]\n" \
  "s_add_u32 s62, s24, s67\ns_addc_u32 s63, s25, 0\ns_lshl_b32 s66, s36, 11\ns_add_u32 s66, s66, 32768\ns_mov_b32 m0, s66\ns_add_u32 s66, s66, 1024\nglobal_load_lds_dwordx4 v97, s[62:63]\n" \
  "s_add_u32 s62, s62, 65536\ns_addc_u32 s63, s63, 0\ns_mov_b32 m0, s66\ns_add_u32 s66, s66, 1024\nglobal_load_lds_dwordx4 v97, s[62:63]\nv_add_u32 v105, 40960, v104\n" \
  "ds_read_b64_tr_b16 v[16:17], v105 offset:0\nds_read_b64_tr_b16 v[18:19], v105 offset:2560\nds_read_b64_tr_b16 v[24:25], v105 offset:32\nds_read_b64_tr_b16 v[26:27], v105 offset:2592\n" \
  "ds_read_b64_tr_b16 v[32:33], v105 offset:64\nds_read_b64_tr_b16 v[34:35], v105 offset:2624\nds_read_b64_tr_b16 v[40:41], v105 offset:96\nds_read_b64_tr_b16 v[42:43], v105 offset:2656\n" \
  "ds_read_b64_tr_b16 v[48:49], v105 offset:128\nds_read_b64_tr_b16 v[50:51], v105 offset:2688\nds_read_b64_tr_b16 v[56:57], v105 offset:160\nds_read_b64_tr_b16 v[58:59], v105 offset:2720\n" \
  "s_waitcnt lgkmcnt(4)\nds_read_b64_tr_b16 v[64:65], v105 offset:192\nds_read_b64_tr_b16 v[66:67], v105 offset:2752\nds_read_b64_tr_b16 v[72:73], v105 offset:224\n" \
  "ds_read_b64_tr_b16 v[74:75], v105 offset:2784\nds_read_b64_tr_b16 v[80:81], v105 offset:256\nds_read_b64_tr_b16 v[82:83], v105 offset:2816\nds_read_b64_tr_b16 v[88:89], v105 offset:288\n" \
  "ds_read_b64_tr_b16 v[90:91], v105 offset:2848\ns_waitcnt vmcnt(20) lgkmcnt(0)\ns_barrier\ns_mov_b32 s67, 1310720\ns_add_u32 s62, s20, s67\ns_addc_u32 s63, s21, 0\n" \
  "s_lshl_b32 s66, s36, 12\ns_add_u32 s66, s66, 40960\ns_mov_b32 m0, s66\ns_add_u32 s66, s66, 1024\nglobal_load_lds_dwordx4 v96, s[62:63]\ns_add_u32 s62, s62, 32768\n" \
  "s_addc_u32 s63, s63, 0\ns_mov_b32 m0, s66\ns_add_u32 s66, s66, 1024\nglobal_load_lds_dwordx4 v96, s[62:63]\ns_add_u32 s62, s62, 32768\ns_addc_u32 s63, s63, 0\n" \
  "s_mov_b32 m0, s66\ns_add_u32 s66, s66, 1024\nglobal_load_lds_dwordx4 v96, s[62:63]\ns_add_u32 s62, s62, 32768\ns_addc_u32 s63, s63, 0\ns_mov_b32 m0, s66\n" \
  "s_add_u32 s66, s66, 1024\nglobal_load_lds_dwordx4 v96, s[62:63]\ns_add_u32 s62, s22, s67\ns_addc_u32 s63, s23, 0\ns_lshl_b32 s66, s36, 12\ns_add_u32 s66, s66, 57344\n" \
  "s_mov_b32 m0, s66\ns_add_u32 s66, s66, 1024\nglobal_load_lds_dwordx4 v96, s[62:63]\ns_add_u32 s62, s62, 32768\ns_addc_u32 s63, s63, 0\ns_mov_b32 m0, s66\n" \
  "s_add_u32 s66, s66, 1024\nglobal_load_lds_dwordx4 v96, s[62:63]\ns_add_u32 s62, s62, 32768\ns_addc_u32 s63, s63, 0\ns_mov_b32 m0, s66\ns_add_u32 s66, s66, 1024\n" \
  "global_load_lds_dwordx4 v96, s[62:63]\ns_add_u32 s62, s62, 32768\ns_addc_u32 s63, s63, 0\ns_mov_b32 m0, s66\ns_add_u32 s66, s66, 1024\nglobal_load_lds_dwordx4 v96, s[62:63]\n" \
  "s_add_u32 s62, s24, s67\ns_addc_u32 s63, s25, 0\ns_lshl_b32 s66, s36, 11\ns_add_u32 s66, s66, 73728\ns_mov_b32 m0, s66\ns_add_u32 s66, s66, 1024\nglobal_load_lds_dwordx4 v97, s[62:63]\n" \
  "s_add_u32 s62, s62, 65536\ns_addc_u32 s63, s63, 0\ns_mov_b32 m0, s66\ns_add_u32 s66, s66, 1024\nglobal_load_lds_dwordx4 v97, s[62:63]\nv_add_u32 v105, 81920, v104\n" \
  "ds_read_b64_tr_b16 v[20:21], v105 offset:0\nds_read_b64_tr_b16 v[22:23], v105 offset:2560\nds_read_b64_tr_b16 v[28:29], v105 offset:32\nds_read_b64_tr_b16 v[30:31], v105 offset:2592\n" \
  "ds_read_b64_tr_b16 v[36:37], v105 offset:64\nds_read_b64_tr_b16 v[38:39], v105 offset:2624\nds_read_b64_tr_b16 v[44:45], v105 offset:96\nds_read_b64_tr_b16 v[46:47], v105 offset:2656\n" \
  "ds_read_b64_tr_b16 v[52:53], v105 offset:128\nds_read_b64_tr_b16 v[54:55], v105 offset:2688\nds_read_b64_tr_b16 v[60:61], v105 offset:160\nds_read_b64_tr_b16 v[62:63], v105 offset:2720\n" \
  "s_waitcnt lgkmcnt(4)\nds_read_b64_tr_b16 v[68:69], v105 offset:192\nds_read_b64_tr_b16 v[70:71], v105 offset:2752\nds_read_b64_tr_b16 v[76:77], v105 offset:224\n" \
  "ds_read_b64_tr_b16 v[78:79], v105 offset:2784\nds_read_b64_tr_b16 v[84:85], v105 offset:256\nds_read_b64_tr_b16 v[86:87], v105 offset:2816\nds_read_b64_tr_b16 v[92:93], v105 offset:288\n" \
  "ds_read_b64_tr_b16 v[94:95], v105 offset:2848\ns_waitcnt vmcnt(20) lgkmcnt(0)\ns_barrier\ns_mov_b32 s67, 1835008\ns_add_u32 s62, s20, s67\ns_addc_u32 s63, s21, 0\n" \
  "s_lshl_b32 s66, s36, 12\ns_add_u32 s66, s66, 81920\ns_mov_b32 m0, s66\ns_add_u32 s66, s66, 1024\nglobal_load_lds_dwordx4 v96, s[62:63]\ns_add_u32 s62, s62, 32768\n" \
  "s_addc_u32 s63, s63, 0\ns_mov_b32 m0, s66\ns_add_u32 s66, s66, 1024\nglobal_load_lds_dwordx4 v96, s[62:63]\ns_add_u32 s62, s62, 32768\ns_addc_u32 s63, s63, 0\n" \
  "s_mov_b32 m0, s66\ns_add_u32 s66, s66, 1024\nglobal_load_lds_dwordx4 v96, s[62:63]\ns_add_u32 s62, s62, 32768\ns_addc_u32 s63, s63, 0\ns_mov_b32 m0, s66\n" \
  "s_add_u32 s66, s66, 1024\nglobal_load_lds_dwordx4 v96, s[62:63]\ns_add_u32 s62, s22, s67\ns_addc_u32 s63, s23, 0\ns_lshl_b32 s66, s36, 12\ns_add_u32 s66, s66, 98304\n" \
  "s_mov_b32 m0, s66\ns_add_u32 s66, s66, 1024\nglobal_load_lds_dwordx4 v96, s[62:63]\ns_add_u32 s62, s62, 32768\ns_addc_u32 s63, s63, 0\ns_mov_b32 m0, s66\n" \
  "s_add_u32 s66, s66, 1024\nglobal_load_lds_dwordx4 v96, s[62:63]\ns_add_u32 s62, s62, 32768\ns_addc_u32 s63, s63, 0\ns_mov_b32 m0, s66\ns_add_u32 s66, s66, 1024\n" \
  "global_load_lds_dwordx4 v96, s[62:63]\ns_add_u32 s62, s62, 32768\ns_addc_u32 s63, s63, 0\ns_mov_b32 m0, s66\ns_add_u32 s66, s66, 1024\nglobal_load_lds_dwordx4 v96, s[62:63]\n" \
  "s_add_u32 s62, s24, s67\ns_addc_u32 s63, s25, 0\ns_lshl_b32 s66, s36, 11\ns_add_u32 s66, s66, 114688\ns_mov_b32 m0, s66\ns_add_u32 s66, s66, 1024\nglobal_load_lds_dwordx4 v97, s[62:63]\n" \
  "s_add_u32 s62, s62, 65536\ns_addc_u32 s63, s63, 0\ns_mov_b32 m0, s66\ns_add_u32 s66, s66, 1024\nglobal_load_lds_dwordx4 v97, s[62:63]\nv_add_u32 v105, 122880, v98\n" \
  "v_add_u32 v106, 122880, v99\nv_add_u32 v107, 122880, v102\nds_read_b64_tr_b16 a[0:1], v105 offset:0\nds_read_b64_tr_b16 a[8:9], v105 offset:16384\nds_read_b64_tr_b16 a[16:17], v106 offset:0\n" \
  "ds_read_b64_tr_b16 a[24:25], v106 offset:16384\nds_read_b64_tr_b16 v[176:177], v107 offset:32768\nds_read_b64_tr_b16 a[2:3], v105 offset:8192\nds_read_b64_tr_b16 a[10:11], v105 offset:24576\n" \
  "ds_read_b64_tr_b16 a[18:19], v106 offset:8192\nds_read_b64_tr_b16 a[26:27], v106 offset:24576\nds_read_b64_tr_b16 v[178:179], v107 offset:36864\nds_read_b64_tr_b16 a[4:5], v105 offset:1024\n" \
  "ds_read_b64_tr_b16 a[12:13], v105 offset:17408\ns_waitcnt lgkmcnt(4)\nds_read_b64_tr_b16 a[20:21], v106 offset:1024\nds_read_b64_tr_b16 a[28:29], v106 offset:17408\n" \
  "ds_read_b64_tr_b16 v[180:181], v107 offset:33280\nds_read_b64_tr_b16 a[6:7], v105 offset:9216\nds_read_b64_tr_b16 a[14:15], v105 offset:25600\nds_read_b64_tr_b16 a[22:23], v106 offset:9216\n" \
  "ds_read_b64_tr_b16 a[30:31], v106 offset:25600\nds_read_b64_tr_b16 v[182:183], v107 offset:37376\ns_waitcnt vmcnt(20) lgkmcnt(0)\ns_barrier\ns_mov_b32 s67, 2621440\n" \
  "s_add_u32 s62, s20, s67\ns_addc_u32 s63, s21, 0\ns_lshl_b32 s66, s36, 12\ns_add_u32 s66, s66, 122880\ns_mov_b32 m0, s66\ns_add_u32 s66, s66, 1024\nglobal_load_lds_dwordx4 v96, s[62:63]\n" \
  "s_add_u32 s62, s62, 32768\ns_addc_u32 s63, s63, 0\ns_mov_b32 m0, s66\ns_add_u32 s66, s66, 1024\nglobal_load_lds_dwordx4 v96, s[62:63]\ns_add_u32 s62, s62, 32768\n" \
  "s_addc_u32 s63, s63, 0\ns_mov_b32 m0, s66\ns_add_u32 s66, s66, 1024\nglobal_load_lds_dwordx4 v96, s[62:63]\ns_add_u32 s62, s62, 32768\ns_addc_u32 s63, s63, 0\n" \
  "s_mov_b32 m0, s66\ns_add_u32 s66, s66, 1024\nglobal_load_lds_dwordx4 v96, s[62:63]\ns_add_u32 s62, s22, s67\ns_addc_u32 s63, s23, 0\ns_lshl_b32 s66, s36, 12\n" \
  "s_add_u32 s66, s66, 139264\ns_mov_b32 m0, s66\ns_add_u32 s66, s66, 1024\nglobal_load_lds_dwordx4 v96, s[62:63]\ns_add_u32 s62, s62, 32768\ns_addc_u32 s63, s63, 0\n" \
  "s_mov_b32 m0, s66\ns_add_u32 s66, s66, 1024\nglobal_load_lds_dwordx4 v96, s[62:63]\ns_add_u32 s62, s62, 32768\ns_addc_u32 s63, s63, 0\ns_mov_b32 m0, s66\n" \
  "s_add_u32 s66, s66, 1024\nglobal_load_lds_dwordx4 v96, s[62:63]\ns_add_u32 s62, s62, 32768\ns_addc_u32 s63, s63, 0\ns_mov_b32 m0, s66\ns_add_u32 s66, s66, 1024\n" \
  "global_load_lds_dwordx4 v96, s[62:63]\ns_add_u32 s62, s24, s67\ns_addc_u32 s63, s25, 0\ns_lshl_b32 s66, s36, 11\ns_add_u32 s66, s66, 155648\ns_mov_b32 m0, s66\n" \
  "s_add_u32 s66, s66, 1024\nglobal_load_lds_dwordx4 v97, s[62:63]\ns_add_u32 s62, s62, 65536\ns_addc_u32 s63, s63, 0\ns_mov_b32 m0, s66\ns_add_u32 s66, s66, 1024\n" \
  "global_load_lds_dwordx4 v97, s[62:63]\nv_add_u32 v105, 0, v98\nv_add_u32 v106, 0, v99\nv_add_u32 v107, 0, v102\nds_read_b64_tr_b16 a[32:33], v105 offset:0\n" \
  "ds_read_b64_tr_b16 a[40:41], v105 offset:16384\nds_read_b64_tr_b16 a[48:49], v106 offset:0\nds_read_b64_tr_b16 a[56:57], v106 offset:16384\nds_read_b64_tr_b16 v[184:185], v107 offset:32768\n" \
  "ds_read_b64_tr_b16 a[34:35], v105 offset:8192\nds_read_b64_tr_b16 a[42:43], v105 offset:24576\nds_read_b64_tr_b16 a[50:51], v106 offset:8192\nds_read_b64_tr_b16 a[58:59], v106 offset:24576\n" \
  "ds_read_b64_tr_b16 v[186:187], v107 offset:36864\nds_read_b64_tr_b16 a[36:37], v105 offset:1024\nds_read_b64_tr_b16 a[44:45], v105 offset:17408\ns_waitcnt lgkmcnt(4)\n" \
  "ds_read_b64_tr_b16 a[52:53], v106 offset:1024\nds_read_b64_tr_b16 a[60:61], v106 offset:17408\nds_read_b64_tr_b16 v[188:189], v107 offset:33280\nds_read_b64_tr_b16 a[38:39], v105 offset:9216\n" \
  "ds_read_b64_tr_b16 a[46:47], v105 offset:25600\nds_read_b64_tr_b16 a[54:55], v106 offset:9216\nds_read_b64_tr_b16 a[62:63], v106 offset:25600\nds_read_b64_tr_b16 v[190:191], v107 offset:37376\n" \
  "s_waitcnt vmcnt(20) lgkmcnt(0)\ns_barrier\ns_mov_b32 s67, 3145728\ns_add_u32 s62, s20, s67\ns_addc_u32 s63, s21, 0\ns_lshl_b32 s66, s36, 12\ns_add_u32 s66, s66, 0\n" \
  "s_mov_b32 m0, s66\ns_add_u32 s66, s66, 1024\nglobal_load_lds_dwordx4 v96, s[62:63]\ns_add_u32 s62, s62, 32768\ns_addc_u32 s63, s63, 0\ns_mov_b32 m0, s66\n" \
  "s_add_u32 s66, s66, 1024\nglobal_load_lds_dwordx4 v96, s[62:63]\ns_add_u32 s62, s62, 32768\ns_addc_u32 s63, s63, 0\ns_mov_b32 m0, s66\ns_add_u32 s66, s66, 1024\n" \
  "global_load_lds_dwordx4 v96, s[62:63]\ns_add_u32 s62, s62, 32768\ns_addc_u32 s63, s63, 0\ns_mov_b32 m0, s66\ns_add_u32 s66, s66, 1024\nglobal_load_lds_dwordx4 v96, s[62:63]\n" \
  "s_add_u32 s62, s22, s67\ns_addc_u32 s63, s23, 0\ns_lshl_b32 s66, s36, 12\ns_add_u32 s66, s66, 16384\ns_mov_b32 m0, s66\ns_add_u32 s66, s66, 1024\nglobal_load_lds_dwordx4 v96, s[62:63]\n" \
  "s_add_u32 s62, s62, 32768\ns_addc_u32 s63, s63, 0\ns_mov_b32 m0, s66\ns_add_u32 s66, s66, 1024\nglobal_load_lds_dwordx4 v96, s[62:63]\ns_add_u32 s62, s62, 32768\n" \
  "s_addc_u32 s63, s63, 0\ns_mov_b32 m0, s66\ns_add_u32 s66, s66, 1024\nglobal_load_lds_dwordx4 v96, s[62:63]\ns_add_u32 s62, s62, 32768\ns_addc_u32 s63, s63, 0\n" \
  "s_mov_b32 m0, s66\ns_add_u32 s66, s66, 1024\nglobal_load_lds_dwordx4 v96, s[62:63]\ns_add_u32 s62, s24, s67\ns_addc_u32 s63, s25, 0\ns_lshl_b32 s66, s36, 11\n" \
  "s_add_u32 s66, s66, 32768\ns_mov_b32 m0, s66\ns_add_u32 s66, s66, 1024\nglobal_load_lds_dwordx4 v97, s[62:63]\ns_add_u32 s62, s62, 65536\ns_addc_u32 s63, s63, 0\n" \
  "s_mov_b32 m0, s66\ns_add_u32 s66, s66, 1024\nglobal_load_lds_dwordx4 v97, s[62:63]\nv_add_u32 v105, 40960, v98\nv_add_u32 v106, 40960, v99\nv_add_u32 v107, 40960, v102\n" \
  "ds_read_b64_tr_b16 a[64:65], v105 offset:0\nds_read_b64_tr_b16 a[72:73], v105 offset:16384\nds_read_b64_tr_b16 a[80:81], v106 offset:0\nds_read_b64_tr_b16 a[88:89], v106 offset:16384\n" \
  "ds_read_b64_tr_b16 v[192:193], v107 offset:32768\nds_read_b64_tr_b16 a[66:67], v105 offset:8192\nds_read_b64_tr_b16 a[74:75], v105 offset:24576\nds_read_b64_tr_b16 a[82:83], v106 offset:8192\n" \
  "ds_read_b64_tr_b16 a[90:91], v106 offset:24576\nds_read_b64_tr_b16 v[194:195], v107 offset:36864\nds_read_b64_tr_b16 a[68:69], v105 offset:1024\nds_read_b64_tr_b16 a[76:77], v105 offset:17408\n" \
  "s_waitcnt lgkmcnt(4)\nds_read_b64_tr_b16 a[84:85], v106 offset:1024\nds_read_b64_tr_b16 a[92:93], v106 offset:17408\nds_read_b64_tr_b16 v[196:197], v107 offset:33280\n" \
  "ds_read_b64_tr_b16 a[70:71], v105 offset:9216\nds_read_b64_tr_b16 a[78:79], v105 offset:25600\nds_read_b64_tr_b16 a[86:87], v106 offset:9216\nds_read_b64_tr_b16 a[94:95], v106 offset:25600\n" \
  "ds_read_b64_tr_b16 v[198:199], v107 offset:37376\ns_waitcnt vmcnt(20) lgkmcnt(0)\ns_barrier\ns_mov_b32 s67, 3932160\ns_add_u32 s62, s20, s67\ns_addc_u32 s63, s21, 0\n" \
  "s_lshl_b32 s66, s36, 12\ns_add_u32 s66, s66, 40960\ns_mov_b32 m0, s66\ns_add_u32 s66, s66, 1024\nglobal_load_lds_dwordx4 v96, s[62:63]\ns_add_u32 s62, s62, 32768\n" \
  "s_addc_u32 s63, s63, 0\ns_mov_b32 m0, s66\ns_add_u32 s66, s66, 1024\nglobal_load_lds_dwordx4 v96, s[62:63]\ns_add_u32 s62, s62, 32768\ns_addc_u32 s63, s63, 0\n" \
  "s_mov_b32 m0, s66\ns_add_u32 s66, s66, 1024\nglobal_load_lds_dwordx4 v96, s[62:63]\ns_add_u32 s62, s62, 32768\ns_addc_u32 s63, s63, 0\ns_mov_b32 m0, s66\n" \
  "s_add_u32 s66, s66, 1024\nglobal_load_lds_dwordx4 v96, s[62:63]\ns_add_u32 s62, s22, s67\ns_addc_u32 s63, s23, 0\ns_lshl_b32 s66, s36, 12\ns_add_u32 s66, s66, 57344\n" \
  "s_mov_b32 m0, s66\ns_add_u32 s66, s66, 1024\nglobal_load_lds_dwordx4 v96, s[62:63]\ns_add_u32 s62, s62, 32768\ns_addc_u32 s63, s63, 0\ns_mov_b32 m0, s66\n" \
  "s_add_u32 s66, s66, 1024\nglobal_load_lds_dwordx4 v96, s[62:63]\ns_add_u32 s62, s62, 32768\ns_addc_u32 s63, s63, 0\ns_mov_b32 m0, s66\ns_add_u32 s66, s66, 1024\n" \
  "global_load_lds_dwordx4 v96, s[62:63]\ns_add_u32 s62, s62, 32768\ns_addc_u32 s63, s63, 0\ns_mov_b32 m0, s66\ns_add_u32 s66, s66, 1024\nglobal_load_lds_dwordx4 v96, s[62:63]\n" \
  "s_add_u32 s62, s24, s67\ns_addc_u32 s63, s25, 0\ns_lshl_b32 s66, s36, 11\ns_add_u32 s66, s66, 73728\ns_mov_b32 m0, s66\ns_add_u32 s66, s66, 1024\nglobal_load_lds_dwordx4 v97, s[62:63]\n" \
  "s_add_u32 s62, s62, 65536\ns_addc_u32 s63, s63, 0\ns_mov_b32 m0, s66\ns_add_u32 s66, s66, 1024\nglobal_load_lds_dwordx4 v97, s[62:63]\nv_add_u32 v105, 81920, v98\n" \
  "v_add_u32 v106, 81920, v99\nv_add_u32 v107, 81920, v102\nds_read_b64_tr_b16 a[96:97], v105 offset:0\nds_read_b64_tr_b16 a[104:105], v105 offset:16384\n" \
  "ds_read_b64_tr_b16 a[112:113], v106 offset:0\nds_read_b64_tr_b16 a[120:121], v106 offset:16384\nds_read_b64_tr_b16 v[200:201], v107 offset:32768\nds_read_b64_tr_b16 a[98:99], v105 offset:8192\n" \
  "ds_read_b64_tr_b16 a[106:107], v105 offset:24576\nds_read_b64_tr_b16 a[114:115], v106 offset:8192\nds_read_b64_tr_b16 a[122:123], v106 offset:24576\nds_read_b64_tr_b16 v[202:203], v107 offset:36864\n" \
  "ds_read_b64_tr_b16 a[100:101], v105 offset:1024\nds_read_b64_tr_b16 a[108:109], v105 offset:17408\ns_waitcnt lgkmcnt(4)\nds_read_b64_tr_b16 a[116:117], v106 offset:1024\n" \
  "ds_read_b64_tr_b16 a[124:125], v106 offset:17408\nds_read_b64_tr_b16 v[204:205], v107 offset:33280\nds_read_b64_tr_b16 a[102:103], v105 offset:9216\nds_read_b64_tr_b16 a[110:111], v105 offset:25600\n" \
  "ds_read_b64_tr_b16 a[118:119], v106 offset:9216\nds_read_b64_tr_b16 a[126:127], v106 offset:25600\nds_read_b64_tr_b16 v[206:207], v107 offset:37376\ns_waitcnt vmcnt(20) lgkmcnt(0)\n" \
  "s_barrier\ns_mov_b32 s67, 4456448\ns_add_u32 s62, s20, s67\ns_addc_u32 s63, s21, 0\ns_lshl_b32 s66, s36, 12\ns_add_u32 s66, s66, 81920\ns_mov_b32 m0, s66\n" \
  "s_add_u32 s66, s66, 1024\nglobal_load_lds_dwordx4 v96, s[62:63]\ns_add_u32 s62, s62, 32768\ns_addc_u32 s63, s63, 0\ns_mov_b32 m0, s66\ns_add_u32 s66, s66, 1024\n" \
  "global_load_lds_dwordx4 v96, s[62:63]\ns_add_u32 s62, s62, 32768\ns_addc_u32 s63, s63, 0\ns_mov_b32 m0, s66\ns_add_u32 s66, s66, 1024\nglobal_load_lds_dwordx4 v96, s[62:63]\n" \
  "s_add_u32 s62, s62, 32768\ns_addc_u32 s63, s63, 0\ns_mov_b32 m0, s66\ns_add_u32 s66, s66, 1024\nglobal_load_lds_dwordx4 v96, s[62:63]\ns_add_u32 s62, s22, s67\n" \
  "s_addc_u32 s63, s23, 0\ns_lshl_b32 s66, s36, 12\ns_add_u32 s66, s66, 98304\ns_mov_b32 m0, s66\ns_add_u32 s66, s66, 1024\nglobal_load_lds_dwordx4 v96, s[62:63]\n" \
  "s_add_u32 s62, s62, 32768\ns_addc_u32 s63, s63, 0\ns_mov_b32 m0, s66\ns_add_u32 s66, s66, 1024\nglobal_load_lds_dwordx4 v96, s[62:63]\ns_add_u32 s62, s62, 32768\n" \
  "s_addc_u32 s63, s63, 0\ns_mov_b32 m0, s66\ns_add_u32 s66, s66, 1024\nglobal_load_lds_dwordx4 v96, s[62:63]\ns_add_u32 s62, s62, 32768\ns_addc_u32 s63, s63, 0\n" \
  "s_mov_b32 m0, s66\ns_add_u32 s66, s66, 1024\nglobal_load_lds_dwordx4 v96, s[62:63]\ns_add_u32 s62, s24, s67\ns_addc_u32 s63, s25, 0\ns_lshl_b32 s66, s36, 11\n" \
  "s_add_u32 s66, s66, 114688\ns_mov_b32 m0, s66\ns_add_u32 s66, s66, 1024\nglobal_load_lds_dwordx4 v97, s[62:63]\ns_add_u32 s62, s62, 65536\ns_addc_u32 s63, s63, 0\n" \
  "s_mov_b32 m0, s66\ns_add_u32 s66, s66, 1024\nglobal_load_lds_dwordx4 v97, s[62:63]\nv_add_u32 v105, 122880, v98\nv_add_u32 v106, 122880, v99\nv_add_u32 v107, 122880, v102\n" \
  "ds_read_b64_tr_b16 a[128:129], v105 offset:0\nds_read_b64_tr_b16 a[136:137], v105 offset:16384\nds_read_b64_tr_b16 a[144:145], v106 offset:0\nds_read_b64_tr_b16 a[152:153], v106 offset:16384\n" \
  "ds_read_b64_tr_b16 v[208:209], v107 offset:32768\nds_read_b64_tr_b16 a[130:131], v105 offset:8192\nds_read_b64_tr_b16 a[138:139], v105 offset:24576\nds_read_b64_tr_b16 a[146:147], v106 offset:8192\n" \
  "ds_read_b64_tr_b16 a[154:155], v106 offset:24576\nds_read_b64_tr_b16 v[210:211], v107 offset:36864\nds_read_b64_tr_b16 a[132:133], v105 offset:1024\nds_read_b64_tr_b16 a[140:141], v105 offset:17408\n" \
  "s_waitcnt lgkmcnt(4)\nds_read_b64_tr_b16 a[148:149], v106 offset:1024\nds_read_b64_tr_b16 a[156:157], v106 offset:17408\nds_read_b64_tr_b16 v[212:213], v107 offset:33280\n" \
  "ds_read_b64_tr_b16 a[134:135], v105 offset:9216\nds_read_b64_tr_b16 a[142:143], v105 offset:25600\nds_read_b64_tr_b16 a[150:151], v106 offset:9216\nds_read_b64_tr_b16 a[158:159], v106 offset:25600\n" \
  "ds_read_b64_tr_b16 v[214:215], v107 offset:37376\ns_waitcnt vmcnt(20) lgkmcnt(0)\ns_barrier\ns_lshr_b32 s67, s36, 1\ns_mul_i32 s67, s67, 1048576\ns_add_u32 s67, s67, 1048576\n" \
  "s_add_u32 s62, s20, s67\ns_addc_u32 s63, s21, 0\ns_lshl_b32 s66, s36, 12\ns_add_u32 s66, s66, 122880\ns_mov_b32 m0, s66\ns_add_u32 s66, s66, 1024\nglobal_load_lds_dwordx4 v96, s[62:63]\n" \
  "s_add_u32 s62, s62, 32768\ns_addc_u32 s63, s63, 0\ns_mov_b32 m0, s66\ns_add_u32 s66, s66, 1024\nglobal_load_lds_dwordx4 v96, s[62:63]\ns_add_u32 s62, s62, 32768\n" \
  "s_addc_u32 s63, s63, 0\ns_mov_b32 m0, s66\ns_add_u32 s66, s66, 1024\nglobal_load_lds_dwordx4 v96, s[62:63]\ns_add_u32 s62, s62, 32768\ns_addc_u32 s63, s63, 0\n" \
  "s_mov_b32 m0, s66\ns_add_u32 s66, s66, 1024\nglobal_load_lds_dwordx4 v96, s[62:63]\ns_add_u32 s62, s22, s67\ns_addc_u32 s63, s23, 0\ns_lshl_b32 s66, s36, 12\n" \
  "s_add_u32 s66, s66, 139264\ns_mov_b32 m0, s66\ns_add_u32 s66, s66, 1024\nglobal_load_lds_dwordx4 v96, s[62:63]\ns_add_u32 s62, s62, 32768\ns_addc_u32 s63, s63, 0\n" \
  "s_mov_b32 m0, s66\ns_add_u32 s66, s66, 1024\nglobal_load_lds_dwordx4 v96, s[62:63]\ns_add_u32 s62, s62, 32768\ns_addc_u32 s63, s63, 0\ns_mov_b32 m0, s66\n" \
  "s_add_u32 s66, s66, 1024\nglobal_load_lds_dwordx4 v96, s[62:63]\ns_add_u32 s62, s62, 32768\ns_addc_u32 s63, s63, 0\ns_mov_b32 m0, s66\ns_add_u32 s66, s66, 1024\n" \
  "global_load_lds_dwordx4 v96, s[62:63]\ns_add_u32 s62, s24, s67\ns_addc_u32 s63, s25, 0\ns_lshl_b32 s66, s36, 11\ns_add_u32 s66, s66, 155648\ns_mov_b32 m0, s66\n" \
  "s_add_u32 s66, s66, 1024\nglobal_load_lds_dwordx4 v97, s[62:63]\ns_add_u32 s62, s62, 65536\ns_addc_u32 s63, s63, 0\ns_mov_b32 m0, s66\ns_add_u32 s66, s66, 1024\n" \
  "global_load_lds_dwordx4 v97, s[62:63]\nv_add_u32 v105, 0, v98\nv_add_u32 v106, 0, v99\nv_add_u32 v107, 0, v102\nds_read_b64_tr_b16 a[160:161], v105 offset:0\n" \
  "ds_read_b64_tr_b16 a[168:169], v105 offset:16384\nds_read_b64_tr_b16 a[176:177], v106 offset:0\nds_read_b64_tr_b16 a[184:185], v106 offset:16384\nds_read_b64_tr_b16 v[216:217], v107 offset:32768\n" \
  "ds_read_b64_tr_b16 a[162:163], v105 offset:8192\nds_read_b64_tr_b16 a[170:171], v105 offset:24576\nds_read_b64_tr_b16 a[178:179], v106 offset:8192\nds_read_b64_tr_b16 a[186:187], v106 offset:24576\n" \
  "ds_read_b64_tr_b16 v[218:219], v107 offset:36864\nds_read_b64_tr_b16 a[164:165], v105 offset:1024\nds_read_b64_tr_b16 a[172:173], v105 offset:17408\ns_waitcnt lgkmcnt(4)\n" \
  "ds_read_b64_tr_b16 a[180:181], v106 offset:1024\nds_read_b64_tr_b16 a[188:189], v106 offset:17408\nds_read_b64_tr_b16 v[220:221], v107 offset:33280\nds_read_b64_tr_b16 a[166:167], v105 offset:9216\n" \
  "ds_read_b64_tr_b16 a[174:175], v105 offset:25600\nds_read_b64_tr_b16 a[182:183], v106 offset:9216\nds_read_b64_tr_b16 a[190:191], v106 offset:25600\nds_read_b64_tr_b16 v[222:223], v107 offset:37376\n" \
  "s_waitcnt vmcnt(20) lgkmcnt(0)\ns_barrier\ns_lshr_b32 s67, s36, 1\ns_mul_i32 s67, s67, 1048576\ns_add_u32 s67, s67, 3670016\ns_add_u32 s62, s20, s67\ns_addc_u32 s63, s21, 0\n" \
  "s_lshl_b32 s66, s36, 12\ns_add_u32 s66, s66, 0\ns_mov_b32 m0, s66\ns_add_u32 s66, s66, 1024\nglobal_load_lds_dwordx4 v96, s[62:63]\ns_add_u32 s62, s62, 32768\n" \
  "s_addc_u32 s63, s63, 0\ns_mov_b32 m0, s66\ns_add_u32 s66, s66, 1024\nglobal_load_lds_dwordx4 v96, s[62:63]\ns_add_u32 s62, s62, 32768\ns_addc_u32 s63, s63, 0\n" \
  "s_mov_b32 m0, s66\ns_add_u32 s66, s66, 1024\nglobal_load_lds_dwordx4 v96, s[62:63]\ns_add_u32 s62, s62, 32768\ns_addc_u32 s63, s63, 0\ns_mov_b32 m0, s66\n" \
  "s_add_u32 s66, s66, 1024\nglobal_load_lds_dwordx4 v96, s[62:63]\ns_add_u32 s62, s22, s67\ns_addc_u32 s63, s23, 0\ns_lshl_b32 s66, s36, 12\ns_add_u32 s66, s66, 16384\n" \
  "s_mov_b32 m0, s66\ns_add_u32 s66, s66, 1024\nglobal_load_lds_dwordx4 v96, s[62:63]\ns_add_u32 s62, s62, 32768\ns_addc_u32 s63, s63, 0\ns_mov_b32 m0, s66\n" \
  "s_add_u32 s66, s66, 1024\nglobal_load_lds_dwordx4 v96, s[62:63]\ns_add_u32 s62, s62, 32768\ns_addc_u32 s63, s63, 0\ns_mov_b32 m0, s66\ns_add_u32 s66, s66, 1024\n" \
  "global_load_lds_dwordx4 v96, s[62:63]\ns_add_u32 s62, s62, 32768\ns_addc_u32 s63, s63, 0\ns_mov_b32 m0, s66\ns_add_u32 s66, s66, 1024\nglobal_load_lds_dwordx4 v96, s[62:63]\n" \
  "s_add_u32 s62, s24, s67\ns_addc_u32 s63, s25, 0\ns_lshl_b32 s66, s36, 11\ns_add_u32 s66, s66, 32768\ns_mov_b32 m0, s66\ns_add_u32 s66, s66, 1024\nglobal_load_lds_dwordx4 v97, s[62:63]\n" \
  "s_add_u32 s62, s62, 65536\ns_addc_u32 s63, s63, 0\ns_mov_b32 m0, s66\ns_add_u32 s66, s66, 1024\nglobal_load_lds_dwordx4 v97, s[62:63]\nv_lshlrev_b32 v152, 3, v1\n" \
  "v_add_u32 v153, 16384, v152\nv_add_u32 v152, 4096, v152\nv_add_u32 v153, 4096, v153\nv_add_u32 v144, 4096, v152\nv_add_u32 v146, 4096, v153\nglobal_load_dwordx2 v[148:149], v152, s[70:71] offset:-4096 sc1\n" \
  "global_load_dwordx2 v[150:151], v153, s[70:71] offset:-4096 sc1\nglobal_load_dwordx2 v[144:145], v144, s[70:71] sc1\nglobal_load_dwordx2 v[146:147], v146, s[70:71] sc1\n" \
  "v_add_u32 v105, 40960, v98\nv_add_u32 v106, 40960, v99\nv_add_u32 v107, 40960, v102\nds_read_b64_tr_b16 a[192:193], v105 offset:0\nds_read_b64_tr_b16 a[200:201], v105 offset:16384\n" \
  "ds_read_b64_tr_b16 a[208:209], v106 offset:0\nds_read_b64_tr_b16 a[216:217], v106 offset:16384\nds_read_b64_tr_b16 v[224:225], v107 offset:32768\nds_read_b64_tr_b16 a[194:195], v105 offset:8192\n" \
  "ds_read_b64_tr_b16 a[202:203], v105 offset:24576\nds_read_b64_tr_b16 a[210:211], v106 offset:8192\nds_read_b64_tr_b16 a[218:219], v106 offset:24576\nds_read_b64_tr_b16 v[226:227], v107 offset:36864\n" \
  "ds_read_b64_tr_b16 a[196:197], v105 offset:1024\nds_read_b64_tr_b16 a[204:205], v105 offset:17408\ns_waitcnt lgkmcnt(4)\nds_read_b64_tr_b16 a[212:213], v106 offset:1024\n" \
  "ds_read_b64_tr_b16 a[220:221], v106 offset:17408\nds_read_b64_tr_b16 v[228:229], v107 offset:33280\nds_read_b64_tr_b16 a[198:199], v105 offset:9216\nds_read_b64_tr_b16 a[206:207], v105 offset:25600\n" \
  "ds_read_b64_tr_b16 a[214:215], v106 offset:9216\nds_read_b64_tr_b16 a[222:223], v106 offset:25600\nds_read_b64_tr_b16 v[230:231], v107 offset:37376\ns_waitcnt vmcnt(24) lgkmcnt(0)\n" \
  "s_barrier\nv_add_u32 v105, 81920, v98\nv_add_u32 v106, 81920, v99\nv_add_u32 v107, 81920, v102\nds_read_b64_tr_b16 a[224:225], v105 offset:0\nds_read_b64_tr_b16 a[232:233], v105 offset:16384\n" \
  "ds_read_b64_tr_b16 a[240:241], v106 offset:0\nds_read_b64_tr_b16 a[248:249], v106 offset:16384\nds_read_b64_tr_b16 v[232:233], v107 offset:32768\nds_read_b64_tr_b16 a[226:227], v105 offset:8192\n" \
  "ds_read_b64_tr_b16 a[234:235], v105 offset:24576\nds_read_b64_tr_b16 a[242:243], v106 offset:8192\nds_read_b64_tr_b16 a[250:251], v106 offset:24576\nds_read_b64_tr_b16 v[234:235], v107 offset:36864\n" \
  "ds_read_b64_tr_b16 a[228:229], v105 offset:1024\nds_read_b64_tr_b16 a[236:237], v105 offset:17408\ns_waitcnt lgkmcnt(4)\nds_read_b64_tr_b16 a[244:245], v106 offset:1024\n" \
  "ds_read_b64_tr_b16 a[252:253], v106 offset:17408\nds_read_b64_tr_b16 v[236:237], v107 offset:33280\nds_read_b64_tr_b16 a[230:231], v105 offset:9216\nds_read_b64_tr_b16 a[238:239], v105 offset:25600\n" \
  "ds_read_b64_tr_b16 a[246:247], v106 offset:9216\nds_read_b64_tr_b16 a[254:255], v106 offset:25600\nds_read_b64_tr_b16 v[238:239], v107 offset:37376\ns_waitcnt vmcnt(14) lgkmcnt(0)\n" \
  "s_barrier\nv_add_u32 v105, 122880, v100\nv_add_u32 v106, 122880, v101\nv_add_u32 v107, 122880, v103\nds_read_b64_tr_b16 v[112:113], v105 offset:0\nds_read_b64_tr_b16 v[120:121], v105 offset:16384\n" \
  "ds_read_b64_tr_b16 v[128:129], v106 offset:0\nds_read_b64_tr_b16 v[136:137], v106 offset:16384\nds_read_b64_tr_b16 v[240:241], v107 offset:32768\nds_read_b64_tr_b16 v[114:115], v106 offset:256\n" \
  "ds_read_b64_tr_b16 v[122:123], v106 offset:16640\nds_read_b64_tr_b16 v[130:131], v105 offset:256\nds_read_b64_tr_b16 v[138:139], v105 offset:16640\nds_read_b64_tr_b16 v[242:243], v107 offset:32896\n" \
  "ds_read_b64_tr_b16 v[116:117], v105 offset:2048\nds_read_b64_tr_b16 v[124:125], v105 offset:18432\ns_waitcnt lgkmcnt(4)\nds_read_b64_tr_b16 v[132:133], v106 offset:2048\n" \
  "ds_read_b64_tr_b16 v[140:141], v106 offset:18432\nds_read_b64_tr_b16 v[244:245], v107 offset:33792\nds_read_b64_tr_b16 v[118:119], v106 offset:2304\nds_read_b64_tr_b16 v[126:127], v106 offset:18688\n" \
  "ds_read_b64_tr_b16 v[134:135], v105 offset:2304\nds_read_b64_tr_b16 v[142:143], v105 offset:18688\nds_read_b64_tr_b16 v[246:247], v107 offset:33920\ns_waitcnt vmcnt(0) lgkmcnt(0)\n" \
  "s_barrier\ns_mov_b32 s62, 0x200000\ns_branch 62f\n60:\nv_add_u32 v144, 4096, v152\nv_add_u32 v146, 4096, v153\nglobal_load_dwordx2 v[148:149], v152, s[70:71] offset:-4096 sc1\n" \
  "global_load_dwordx2 v[150:151], v153, s[70:71] offset:-4096 sc1\nglobal_load_dwordx2 v[144:145], v144, s[70:71] sc1\nglobal_load_dwordx2 v[146:147], v146, s[70:71] sc1\n" \
  "s_waitcnt vmcnt(0)\n62:\nv_xor_b32 v144, s72, v144\nv_xor_b32 v145, s73, v145\nv_xor_b32 v146, s72, v146\nv_xor_b32 v147, s73, v147\nv_xor_b32 v148, s72, v148\n" \
  "v_xor_b32 v149, s73, v149\nv_xor_b32 v150, s72, v150\nv_xor_b32 v151, s73, v151\nv_or3_b32 v145, v145, v147, v149\nv_or3_b32 v147, v144, v146, v148\nv_or_b32 v147, v147, v150\n" \
  "v_lshrrev_b32 v149, 5, v147\nv_or3_b32 v145, v145, v149, v151\nv_cmp_ne_u32 vcc, 0, v145\ns_cbranch_vccz 61f\ns_sleep 4\ns_sub_u32 s62, s62, 1\ns_cbranch_scc0 60b\n" \
  "s_branch 90f\n61:\nv_cmp_lt_u32 vcc, 3, v147\ns_mov_b32 s95, 1\ns_cbranch_vccnz 93f\ns_mov_b32 s95, 0\nv_and_b32 v144, 1, v144\nv_cmp_ne_u32 vcc, 0, v144\n" \
  "s_nop 3\ns_bcnt1_i32_b64 s62, vcc\ns_add_u32 s95, s95, s62\nv_and_b32 v146, 1, v146\nv_cmp_ne_u32 vcc, 0, v146\ns_nop 3\ns_bcnt1_i32_b64 s62, vcc\ns_add_u32 s95, s95, s62\n" \
  "v_and_b32 v148, 1, v148\nv_cmp_ne_u32 vcc, 0, v148\ns_nop 3\ns_bcnt1_i32_b64 s62, vcc\ns_add_u32 s95, s95, s62\nv_and_b32 v150, 1, v150\nv_cmp_ne_u32 vcc, 0, v150\n" \
  "s_nop 3\ns_bcnt1_i32_b64 s62, vcc\ns_add_u32 s95, s95, s62\ns_cmp_gt_u32 s95, 28\ns_cselect_b32 s95, 0x2000, 0\ns_add_u32 s76, s44, 0x602000\ns_addc_u32 s77, s45, 0\n" \
  "s_xor_b32 s68, s68, s95\ns_xor_b32 s44, s44, s95\ns_xor_b32 s46, s46, s95\ns_xor_b32 s76, s76, s95\ns_xor_b32 s52, s52, s95\ns_xor_b32 s54, s54, s95\ns_xor_b32 s28, s28, s95\n" \
  "s_xor_b32 s30, s30, s95\nv_mov_b32 v109, s14\ns_add_u32 s62, s14, 0\nv_writelane_b32 v109, s62, 0\ns_add_u32 s62, s15, 0\nv_writelane_b32 v109, s62, 1\n" \
  "s_add_u32 s62, s94, 0\nv_writelane_b32 v109, s62, 2\ns_add_u32 s62, s14, 97280\nv_writelane_b32 v109, s62, 3\ns_add_u32 s62, s15, 97280\nv_writelane_b32 v109, s62, 4\n" \
  "s_add_u32 s62, s94, 97280\nv_writelane_b32 v109, s62, 5\ns_add_u32 s62, s14, 113664\nv_writelane_b32 v109, s62, 6\ns_add_u32 s62, s15, 113664\nv_writelane_b32 v109, s62, 7\n" \
  "s_add_u32 s62, s94, 113664\nv_writelane_b32 v109, s62, 8\nglobal_load_dword v111, v109, s[12:13] sc1\nv_mov_b64 v[144:145], 0\nv_mov_b64 v[146:147], 0\n" \
  "s_add_u32 s62, s76, 0x20000\ns_addc_u32 s63, s77, 0\nglobal_store_dwordx4 v5, v[144:147], s[44:45] sc1\nglobal_store_dwordx4 v5, v[144:147], s[46:47] sc1\n" \
  "global_store_dwordx4 v5, v[144:147], s[76:77] sc1\nglobal_store_dwordx4 v5, v[144:147], s[62:63] sc1\nglobal_store_dwordx2 v6, v[144:145], s[44:45] sc1\n" \
  "global_store_dwordx2 v6, v[144:145], s[76:77] sc1\ns_nop 1\nv_add_u32 v105, 0, v100\nv_add_u32 v106, 0, v101\nv_add_u32 v107, 0, v103\nds_read_b64_tr_b16 v[144:145], v105 offset:0\n" \
  "ds_read_b64_tr_b16 v[152:153], v105 offset:16384\nds_read_b64_tr_b16 v[160:161], v106 offset:0\nds_read_b64_tr_b16 v[168:169], v106 offset:16384\nds_read_b64_tr_b16 v[248:249], v107 offset:32768\n" \
  "ds_read_b64_tr_b16 v[146:147], v106 offset:256\nds_read_b64_tr_b16 v[154:155], v106 offset:16640\nds_read_b64_tr_b16 v[162:163], v105 offset:256\nds_read_b64_tr_b16 v[170:171], v105 offset:16640\n" \
  "ds_read_b64_tr_b16 v[250:251], v107 offset:32896\nds_read_b64_tr_b16 v[148:149], v105 offset:2048\nds_read_b64_tr_b16 v[156:157], v105 offset:18432\ns_waitcnt lgkmcnt(4)\n" \
  "ds_read_b64_tr_b16 v[164:165], v106 offset:2048\nds_read_b64_tr_b16 v[172:173], v106 offset:18432\nds_read_b64_tr_b16 v[252:253], v107 offset:33792\nds_read_b64_tr_b16 v[150:151], v106 offset:2304\n" \
  "ds_read_b64_tr_b16 v[158:159], v106 offset:18688\nds_read_b64_tr_b16 v[166:167], v105 offset:2304\nds_read_b64_tr_b16 v[174:175], v105 offset:18688\nds_read_b64_tr_b16 v[254:255], v107 offset:33920\n" \
  "s_waitcnt vmcnt(6) lgkmcnt(0)\ns_lshl_b32 s66, s36, 11\ns_lshl_b32 s67, s36, 9\ns_add_u32 s67, s67, 8192\ns_barrier\ns_mov_b32 m0, s66\ns_nop 0\nglobal_load_lds_dwordx4 v1, s[40:41] sc1\n" \
  "global_load_lds_dwordx4 v1, s[40:41] offset:1024 sc1\ns_mov_b32 m0, s67\ns_mov_b32 exec_hi, 0\nglobal_load_lds_dwordx4 v1, s[42:43] sc1\ns_mov_b32 exec_hi, -1\n" \
  "v_cmp_eq_u32 vcc, 0, v111\ns_cbranch_vccnz 43f\ns_mov_b32 m0, s87\ns_xor_b32 s87, s87, s89\nglobal_load_lds_dwordx4 v1, s[48:49] sc1\nglobal_load_lds_dwordx4 v1, s[48:49] offset:1024 sc1\n" \
  "s_mov_b32 m0, s88\ns_xor_b32 s88, s88, s61\nglobal_load_lds_dwordx4 v1, s[50:51] sc1\ns_add_u32 s48, s48, 524288\ns_addc_u32 s49, s49, 0\ns_lshr_b32 s35, s48, 8\n" \
  "s_or_b32 s48, s48, 0x2000\ns_and_b32 s35, s35, 0x2000\ns_nop 0\ns_xor_b32 s48, s48, s35\ns_add_u32 s50, s50, 524288\ns_addc_u32 s51, s51, 0\ns_lshr_b32 s35, s50, 8\n" \
  "s_or_b32 s50, s50, 0x2000\ns_and_b32 s35, s35, 0x2000\ns_nop 0\ns_xor_b32 s50, s50, s35\ns_mov_b32 m0, s87\ns_xor_b32 s87, s87, s89\nglobal_load_lds_dwordx4 v1, s[48:49] sc1\n" \
  "global_load_lds_dwordx4 v1, s[48:49] offset:1024 sc1\ns_mov_b32 m0, s88\ns_xor_b32 s88, s88, s61\nglobal_load_lds_dwordx4 v1, s[50:51] sc1\ns_add_u32 s48, s48, 524288\n" \
  "s_addc_u32 s49, s49, 0\ns_lshr_b32 s35, s48, 8\ns_or_b32 s48, s48, 0x2000\ns_and_b32 s35, s35, 0x2000\ns_nop 0\ns_xor_b32 s48, s48, s35\ns_add_u32 s50, s50, 524288\n" \
  "s_addc_u32 s51, s51, 0\ns_lshr_b32 s35, s50, 8\ns_or_b32 s50, s50, 0x2000\ns_and_b32 s35, s35, 0x2000\ns_nop 0\ns_xor_b32 s50, s50, s35\nds_write_b128 v11, v[16:19] offset:0\n" \
  "ds_write_b128 v11, v[20:23] offset:1024\nds_write_b128 v11, v[24:27] offset:2048\nds_write_b128 v11, v[28:31] offset:3072\nds_write_b128 v11, v[32:35] offset:4096\n" \
  "ds_write_b128 v11, v[36:39] offset:5120\nds_write_b128 v11, v[40:43] offset:6144\nds_write_b128 v11, v[44:47] offset:7168\ns_waitcnt lgkmcnt(4)\nds_write_b128 v11, v[48:51] offset:8192\n" \
  "ds_write_b128 v11, v[52:55] offset:9216\nds_write_b128 v11, v[56:59] offset:10240\nds_write_b128 v11, v[60:63] offset:11264\nds_write_b128 v11, v[64:67] offset:12288\n" \
  "ds_write_b128 v11, v[68:71] offset:13312\nds_write_b128 v11, v[72:75] offset:14336\nds_write_b128 v11, v[76:79] offset:15360\ns_waitcnt lgkmcnt(4)\nds_write_b128 v11, v[80:83] offset:16384\n" \
  "ds_write_b128 v11, v[84:87] offset:17408\nds_write_b128 v11, v[88:91] offset:18432\nds_write_b128 v11, v[92:95] offset:19456\nds_write_b128 v11, v[216:219] offset:20480\n" \
  "ds_write_b128 v11, v[220:223] offset:21504\nds_write_b128 v11, v[224:227] offset:22528\nds_write_b128 v11, v[228:231] offset:23552\nds_write_b128 v11, v[232:235] offset:24576\n" \
  "ds_write_b128 v11, v[236:239] offset:25600\ns_waitcnt lgkmcnt(4)\nds_write_b128 v11, v[240:243] offset:26624\nds_write_b128 v11, v[244:247] offset:27648\n" \
  "ds_write_b128 v11, v[248:251] offset:28672\nds_write_b128 v11, v[252:255] offset:29696\ns_branch 42f\n43:\nds_write_b128 v11, v[16:19] offset:0\nds_write_b128 v11, v[20:23] offset:1024\n" \
  "ds_write_b128 v11, v[24:27] offset:2048\nds_write_b128 v11, v[28:31] offset:3072\nds_write_b128 v11, v[32:35] offset:4096\nds_write_b128 v11, v[36:39] offset:5120\n" \
  "ds_write_b128 v11, v[40:43] offset:6144\nds_write_b128 v11, v[44:47] offset:7168\ns_waitcnt lgkmcnt(4)\nds_write_b128 v11, v[48:51] offset:8192\nds_write_b128 v11, v[52:55] offset:9216\n" \
  "ds_write_b128 v11, v[56:59] offset:10240\nds_write_b128 v11, v[60:63] offset:11264\nds_write_b128 v11, v[64:67] offset:12288\nds_write_b128 v11, v[68:71] offset:13312\n" \
  "ds_write_b128 v11, v[72:75] offset:14336\nds_write_b128 v11, v[76:79] offset:15360\ns_waitcnt lgkmcnt(4)\nds_write_b128 v11, v[80:83] offset:16384\nds_write_b128 v11, v[84:87] offset:17408\n" \
  "ds_write_b128 v11, v[88:91] offset:18432\nds_write_b128 v11, v[92:95] offset:19456\nds_write_b128 v11, v[216:219] offset:20480\nds_write_b128 v11, v[220:223] offset:21504\n" \
  "ds_write_b128 v11, v[224:227] offset:22528\nds_write_b128 v11, v[228:231] offset:23552\nds_write_b128 v11, v[232:235] offset:24576\nds_write_b128 v11, v[236:239] offset:25600\n" \
  "s_waitcnt lgkmcnt(4)\nds_write_b128 v11, v[240:243] offset:26624\nds_write_b128 v11, v[244:247] offset:27648\nds_write_b128 v11, v[248:251] offset:28672\n" \
  "ds_write_b128 v11, v[252:255] offset:29696\n40:\nglobal_load_dword v111, v109, s[12:13] sc1\ns_waitcnt vmcnt(0)\nv_cmp_eq_u32 vcc, 0, v111\ns_cbranch_vccz 41f\n" \
  "s_sleep 2\ns_sub_u32 s58, s58, 1\ns_cbranch_scc0 40b\ns_branch 90f\n41:\ns_mov_b32 m0, s87\ns_xor_b32 s87, s87, s89\nglobal_load_lds_dwordx4 v1, s[48:49] sc1\n" \
  "global_load_lds_dwordx4 v1, s[48:49] offset:1024 sc1\ns_mov_b32 m0, s88\ns_xor_b32 s88, s88, s61\nglobal_load_lds_dwordx4 v1, s[50:51] sc1\ns_add_u32 s48, s48, 524288\n" \
  "s_addc_u32 s49, s49, 0\ns_lshr_b32 s35, s48, 8\ns_or_b32 s48, s48, 0x2000\ns_and_b32 s35, s35, 0x2000\ns_nop 0\ns_xor_b32 s48, s48, s35\ns_add_u32 s50, s50, 524288\n" \
  "s_addc_u32 s51, s51, 0\ns_lshr_b32 s35, s50, 8\ns_or_b32 s50, s50, 0x2000\ns_and_b32 s35, s35, 0x2000\ns_nop 0\ns_xor_b32 s50, s50, s35\ns_mov_b32 m0, s87\n" \
  "s_xor_b32 s87, s87, s89\nglobal_load_lds_dwordx4 v1, s[48:49] sc1\nglobal_load_lds_dwordx4 v1, s[48:49] offset:1024 sc1\ns_mov_b32 m0, s88\ns_xor_b32 s88, s88, s61\n" \
  "global_load_lds_dwordx4 v1, s[50:51] sc1\ns_add_u32 s48, s48, 524288\ns_addc_u32 s49, s49, 0\ns_lshr_b32 s35, s48, 8\ns_or_b32 s48, s48, 0x2000\ns_and_b32 s35, s35, 0x2000\n" \
  "s_nop 0\ns_xor_b32 s48, s48, s35\ns_add_u32 s50, s50, 524288\ns_addc_u32 s51, s51, 0\ns_lshr_b32 s35, s50, 8\ns_or_b32 s50, s50, 0x2000\ns_and_b32 s35, s35, 0x2000\n" \
  "s_nop 0\ns_xor_b32 s50, s50, s35\n42:\ns_waitcnt vmcnt(0) lgkmcnt(0)\ns_barrier\ns_lshl_b32 s62, s36, 11\ns_sub_u32 s62, s87, s62\nv_add_u32 v38, s62, v1\n" \
  "v_add_u32 v39, s88, v1\nds_read_b128 v[76:79], v38 offset:0\nds_read_b128 v[80:83], v38 offset:1024\nds_read_b128 v[84:87], v38 offset:2048\nds_read_b128 v[88:91], v38 offset:3072\n" \
  "ds_read_b128 v[92:95], v38 offset:4096\nds_read_b128 v[96:99], v38 offset:5120\nds_read_b128 v[100:103], v38 offset:6144\nds_read_b128 v[104:107], v38 offset:7168\n" \
  "ds_read_b128 v[108:111], v39\ns_waitcnt lgkmcnt(4)\ns_waitcnt lgkmcnt(0)\ns_barrier\ns_mov_b32 m0, s87\ns_xor_b32 s87, s87, s89\nglobal_load_lds_dwordx4 v1, s[48:49] sc1\n" \
  "global_load_lds_dwordx4 v1, s[48:49] offset:1024 sc1\ns_mov_b32 m0, s88\ns_xor_b32 s88, s88, s61\nglobal_load_lds_dwordx4 v1, s[50:51] sc1\nv_mov_b64 v[56:57], 0\n" \
  "v_mov_b64 v[58:59], 0\nv_mov_b64 v[60:61], 0\nv_mov_b64 v[62:63], 0\nv_mov_b64 v[64:65], 0\nv_mov_b64 v[66:67], 0\nv_mov_b64 v[68:69], 0\nv_mov_b64 v[70:71], 0\n" \
  "v_mov_b64 v[72:73], 0\nv_mov_b64 v[74:75], 0\ns_mov_b32 s16, s59\ns_mov_b32 s17, s59\ns_add_u32 s37, s34, 40\ns_mov_b32 s65, 0\ns_memrealtime s[62:63]\n" \
  "s_waitcnt lgkmcnt(0)\ns_sub_u32 s64, s62, 1000\ns_branch 12f\n15:\ns_nop 7\nv_mov_b64 v[56:57], 0\nv_mov_b64 v[58:59], 0\nv_mov_b64 v[60:61], 0\nv_mov_b64 v[62:63], 0\n" \
  "v_mov_b64 v[64:65], 0\nv_mov_b64 v[66:67], 0\nv_mov_b64 v[68:69], 0\nv_mov_b64 v[70:71], 0\nv_mov_b64 v[72:73], 0\nv_mov_b64 v[74:75], 0\ns_waitcnt vmcnt(0) lgkmcnt(0)\n" \
  "s_barrier\n11:\ns_mov_b32 m0, s66\ns_nop 0\nglobal_load_lds_dwordx4 v1, s[40:41] sc1\nglobal_load_lds_dwordx4 v1, s[40:41] offset:1024 sc1\ns_mov_b32 m0, s67\n" \
  "s_mov_b32 exec_hi, 0\nglobal_load_lds_dwordx4 v1, s[42:43] sc1\ns_mov_b32 exec_hi, -1\ns_sub_u32 s58, s58, 1\ns_cbranch_scc1 90f\ns_waitcnt vmcnt(0) lgkmcnt(0)\n" \
  "s_branch 12f\n10:\ns_memrealtime s[62:63]\ns_waitcnt vmcnt(2) lgkmcnt(0)\ns_branch 34f\n30:\ns_mov_b64 exec, 0xffff\nglobal_load_dword v41, v43, s[30:31] sc1\n" \
  "s_mov_b64 exec, -1\ns_memrealtime s[62:63]\ns_waitcnt vmcnt(0) lgkmcnt(0)\n34:\ns_mov_b64 exec, 0xffff\nv_and_b32 v40, 127, v41\ns_lshl_b32 s63, s62, 7\n" \
  "v_cmp_gt_u32 vcc, s56, v40\ns_mov_b64 exec, -1\ns_cbranch_vccz 31f\ns_sleep 3\ns_sub_u32 s58, s58, 1\ns_cbranch_scc0 30b\ns_branch 90f\n31:\nv_subrev_u32 v40, s63, v41\n" \
  "s_nop 1\nv_max_i32_dpp v40, v40, v40 row_ror:1 row_mask:0xf bank_mask:0xf\ns_nop 1\nv_max_i32_dpp v40, v40, v40 row_ror:2 row_mask:0xf bank_mask:0xf\ns_nop 1\n" \
  "v_max_i32_dpp v40, v40, v40 row_ror:4 row_mask:0xf bank_mask:0xf\ns_nop 1\nv_max_i32_dpp v40, v40, v40 row_ror:8 row_mask:0xf bank_mask:0xf\nv_ashrrev_i32 v40, 7, v40\n" \
  "s_lshl_b32 s63, s36, 2\nv_add_u32 v40, s62, v40\nv_mov_b32 v41, s63\ns_mov_b64 exec, 1\nds_write_b32 v41, v40 offset:16320\ns_mov_b64 exec, -1\nv_mov_b32 v41, 0\n" \
  "s_waitcnt lgkmcnt(0)\ns_barrier\nds_read_b128 v[48:51], v41 offset:16320\ns_add_u32 s35, s56, 3\ns_min_u32 s35, s35, 64\ns_branch 49f\n48:\nglobal_load_dword v47, v46, s[12:13] sc1\n" \
  "s_waitcnt vmcnt(0)\n49:\nv_cmp_gt_u32 vcc, s35, v47\ns_cbranch_vccz 50f\ns_sleep 4\ns_sub_u32 s58, s58, 1\ns_cbranch_scc0 48b\ns_branch 90f\n50:\ns_cmp_eq_u32 s56, 1\n" \
  "s_cbranch_scc0 55f\ns_xor_b32 s40, s40, s95\nv_lshlrev_b32 v46, 3, v1\nv_add_u32 v46, 76800, v46\n52:\nglobal_load_dword v47, v46, s[12:13] sc1\ns_waitcnt vmcnt(0)\n" \
  "53:\nv_cmp_gt_u32 vcc, 1, v47\ns_cbranch_vccz 54f\ns_sleep 4\ns_sub_u32 s58, s58, 1\ns_cbranch_scc0 52b\ns_branch 90f\n54:\n55:\ns_waitcnt lgkmcnt(0)\n" \
  "v_subrev_u32 v48, s62, v48\nv_subrev_u32 v49, s62, v49\nv_subrev_u32 v50, s62, v50\nv_subrev_u32 v51, s62, v51\nv_max3_i32 v48, v48, v49, v50\nv_max_i32 v48, v48, v51\n" \
  "s_nop 0\nv_readfirstlane_b32 s63, v48\ns_nop 3\ns_add_u32 s63, s63, s62\ns_sub_u32 s64, s63, s64\ns_cmp_gt_i32 s64, 52\ns_cselect_b32 s64, 16, 0\ns_add_u32 s64, s64, s63\n" \
  "s_add_u32 s64, s64, s34\n35:\ns_memrealtime s[62:63]\ns_waitcnt lgkmcnt(0)\ns_sub_u32 s63, s62, s64\ns_cmp_ge_i32 s63, -8\ns_cbranch_scc1 36f\ns_memrealtime s[62:63]\n" \
  "s_waitcnt lgkmcnt(0)\ns_sub_u32 s63, s62, s64\ns_cmp_ge_i32 s63, -8\ns_cbranch_scc1 36f\ns_memrealtime s[62:63]\ns_waitcnt lgkmcnt(0)\ns_sub_u32 s63, s62, s64\n" \
  "s_cmp_ge_i32 s63, -8\ns_cbranch_scc1 36f\ns_memrealtime s[62:63]\ns_waitcnt lgkmcnt(0)\ns_sub_u32 s63, s62, s64\ns_cmp_ge_i32 s63, -8\ns_cbranch_scc1 36f\n" \
  "s_branch 35b\n36:\ns_mov_b32 m0, s66\ns_nop 0\nglobal_load_lds_dwordx4 v1, s[40:41] sc1\n32:\ns_memrealtime s[62:63]\ns_waitcnt lgkmcnt(0)\ns_sub_u32 s63, s62, s64\n" \
  "s_cmp_ge_i32 s63, 0\ns_cbranch_scc1 33f\ns_memrealtime s[62:63]\ns_waitcnt lgkmcnt(0)\ns_sub_u32 s63, s62, s64\ns_cmp_ge_i32 s63, 0\ns_cbranch_scc1 33f\n" \
  "s_memrealtime s[62:63]\ns_waitcnt lgkmcnt(0)\ns_sub_u32 s63, s62, s64\ns_cmp_ge_i32 s63, 0\ns_cbranch_scc1 33f\ns_memrealtime s[62:63]\ns_waitcnt lgkmcnt(0)\n" \
  "s_sub_u32 s63, s62, s64\ns_cmp_ge_i32 s63, 0\ns_cbranch_scc1 33f\ns_branch 32b\n33:\nglobal_load_lds_dwordx4 v1, s[40:41] offset:1024 sc1\ns_mov_b32 m0, s67\n" \
  "s_mov_b32 exec_hi, 0\nglobal_load_lds_dwordx4 v1, s[42:43] sc1\ns_mov_b32 exec_hi, -1\ns_sub_u32 s58, s58, 1\ns_cbranch_scc1 90f\ns_mov_b32 s65, s58\nv_mov_b32 v45, s56\n" \
  "s_sleep 3\ns_waitcnt vmcnt(3)\ns_mov_b64 exec, 1\nglobal_store_dword v1, v45, s[10:11] sc1\ns_mov_b64 exec, -1\ns_mov_b32 m0, s87\ns_xor_b32 s87, s87, s89\n" \
  "global_load_lds_dwordx4 v1, s[48:49] sc1\nglobal_load_lds_dwordx4 v1, s[48:49] offset:1024 sc1\ns_mov_b32 m0, s88\ns_xor_b32 s88, s88, s61\nglobal_load_lds_dwordx4 v1, s[50:51] sc1\n" \
  "s_waitcnt vmcnt(6) lgkmcnt(0)\n12:\ns_barrier\nds_read_b128 v[16:19], v4 offset:0\nds_read_b128 v[24:27], v4 offset:2048\nds_read_b128 v[32:35], v4 offset:4096\n" \
  "ds_read_b128 v[40:43], v4 offset:6144\ns_waitcnt lgkmcnt(3)\nv_smfmac_f32_16x16x64_bf16 v[56:59], v[16:19], a[0:7], v0\nv_max_u32 v2, v16, v18\nv_smfmac_f32_16x16x64_bf16 v[60:63], v[16:19], a[8:15], v0\n" \
  "v_cmp_eq_u32 vcc, -1, v2\nv_smfmac_f32_16x16x64_bf16 v[64:67], v[16:19], a[16:23], v0\ns_cbranch_vccnz 15b\nv_smfmac_f32_16x16x64_bf16 v[68:71], v[16:19], a[24:31], v0\n" \
  "s_waitcnt lgkmcnt(2)\nv_smfmac_f32_16x16x64_bf16 v[56:59], v[24:27], a[64:71], v0\nv_max3_u32 v2, v2, v24, v26\nv_smfmac_f32_16x16x64_bf16 v[60:63], v[24:27], a[72:79], v0\n" \
  "v_smfmac_f32_16x16x64_bf16 v[64:67], v[24:27], a[80:87], v0\nv_smfmac_f32_16x16x64_bf16 v[68:71], v[24:27], a[88:95], v0\ns_waitcnt lgkmcnt(0)\nv_smfmac_f32_16x16x64_bf16 v[56:59], v[32:35], a[128:135], v0\n" \
  "v_max3_u32 v2, v2, v32, v34\nv_smfmac_f32_16x16x64_bf16 v[60:63], v[32:35], a[136:143], v0\nv_max3_u32 v2, v2, v40, v42\nv_smfmac_f32_16x16x64_bf16 v[64:67], v[32:35], a[144:151], v0\n" \
  "v_smfmac_f32_16x16x64_bf16 v[68:71], v[32:35], a[152:159], v0\ns_waitcnt vmcnt(4)\ns_barrier\nv_smfmac_f32_16x16x64_bf16 v[56:59], v[40:43], a[192:199], v0\n" \
  "ds_read_b128 v[20:23], v4 offset:1024\ns_memrealtime s[82:83]\nv_smfmac_f32_16x16x64_bf16 v[60:63], v[40:43], a[200:207], v0\nds_read_b128 v[28:31], v4 offset:3072\n" \
  "v_smfmac_f32_16x16x64_bf16 v[64:67], v[40:43], a[208:215], v0\nds_read_b128 v[36:39], v4 offset:5120\nv_smfmac_f32_16x16x64_bf16 v[68:71], v[40:43], a[216:223], v0\n" \
  "ds_read_b128 v[44:47], v4 offset:7168\ns_waitcnt lgkmcnt(3)\nv_smfmac_f32_16x16x64_bf16 v[56:59], v[20:23], a[32:39], v0\nv_max3_u32 v2, v2, v20, v22\n" \
  "v_smfmac_f32_16x16x64_bf16 v[60:63], v[20:23], a[40:47], v0\nds_read_b128 v[48:51], v4 offset:8192\nv_smfmac_f32_16x16x64_bf16 v[64:67], v[20:23], a[48:55], v0\n" \
  "ds_read_b128 v[52:55], v4 offset:9216\nv_smfmac_f32_16x16x64_bf16 v[68:71], v[20:23], a[56:63], v0\ns_waitcnt lgkmcnt(4)\nv_smfmac_f32_16x16x64_bf16 v[56:59], v[28:31], a[96:103], v0\n" \
  "v_max3_u32 v2, v2, v28, v30\nv_smfmac_f32_16x16x64_bf16 v[60:63], v[28:31], a[104:111], v0\nv_smfmac_f32_16x16x64_bf16 v[64:67], v[28:31], a[112:119], v0\n" \
  "v_smfmac_f32_16x16x64_bf16 v[68:71], v[28:31], a[120:127], v0\ns_waitcnt lgkmcnt(0)\nv_smfmac_f32_16x16x64_bf16 v[56:59], v[36:39], a[160:167], v0\nv_max3_u32 v2, v2, v36, v38\n" \
  "v_smfmac_f32_16x16x64_bf16 v[60:63], v[36:39], a[168:175], v0\nv_max3_u32 v2, v2, v44, v46\nv_smfmac_f32_16x16x64_bf16 v[64:67], v[36:39], a[176:183], v0\n" \
  "v_max3_u32 v2, v2, v48, v50\nv_smfmac_f32_16x16x64_bf16 v[68:71], v[36:39], a[184:191], v0\nv_max3_u32 v2, v2, v52, v54\nv_smfmac_f32_16x16x64_bf16 v[56:59], v[44:47], a[224:231], v0\n" \
  "v_cmp_eq_u32 vcc, -1, v2\nv_smfmac_f32_16x16x64_bf16 v[60:63], v[44:47], a[232:239], v0\ns_lshl_b32 s82, s82, 7\nv_smfmac_f32_16x16x64_bf16 v[64:67], v[44:47], a[240:247], v0\n" \
  "s_cbranch_vccnz 15b\nv_smfmac_f32_16x16x64_bf16 v[68:71], v[44:47], a[248:255], v0\ns_add_u32 s82, s82, s56\nv_smfmac_f32_16x16x64_bf16 v[56:59], v[48:51], v[112:119], v0\n" \
  "v_add_u32 v3, 1, s82\nv_smfmac_f32_16x16x64_bf16 v[60:63], v[48:51], v[120:127], v0\ns_mov_b64 exec, 1\nglobal_store_dword v1, v3, s[28:29] sc1\ns_mov_b64 exec, -1\n" \
  "v_smfmac_f32_16x16x64_bf16 v[56:59], v[52:55], v[144:151], v0\nv_add_u32 v2, s60, v8\nv_smfmac_f32_16x16x64_bf16 v[60:63], v[52:55], v[152:159], v0\nv_smfmac_f32_16x16x64_bf16 v[64:67], v[48:51], v[128:135], v0\n" \
  "s_nop 0\nv_smfmac_f32_16x16x64_bf16 v[68:71], v[48:51], v[136:143], v0\ns_nop 1\nv_smfmac_f32_16x16x64_bf16 v[64:67], v[52:55], v[160:167], v0\nv_add_f32 v56, v56, v57\n" \
  "v_add_f32 v58, v58, v59\nv_smfmac_f32_16x16x64_bf16 v[68:71], v[52:55], v[168:175], v0\nv_add_f32 v60, v60, v61\nv_add_f32 v62, v62, v63\nv_cvt_pk_bf16_f32 v61, v56, v58\n" \
  "v_cvt_pk_bf16_f32 v60, v60, v62\nds_write_b64 v7, v[60:61]\nv_add_f32 v64, v64, v65\nv_add_f32 v66, v66, v67\nv_cvt_pk_bf16_f32 v65, v64, v66\nv_add_f32 v68, v68, v69\n" \
  "v_add_f32 v70, v70, v71\nv_cvt_pk_bf16_f32 v64, v68, v70\nds_write_b64 v7, v[64:65] offset:512\nv_smfmac_f32_16x16x64_bf16 v[72:75], v[16:19], v[176:183], v0\n" \
  "v_smfmac_f32_16x16x64_bf16 v[72:75], v[20:23], v[184:191], v0\nv_smfmac_f32_16x16x64_bf16 v[72:75], v[24:27], v[192:199], v0\nv_smfmac_f32_16x16x64_bf16 v[72:75], v[28:31], v[200:207], v0\n" \
  "s_waitcnt lgkmcnt(0)\ns_barrier\nds_read_b64_tr_b16 v[16:17], v8 offset:0\nds_read_b64_tr_b16 v[18:19], v8 offset:512\nds_read_b64_tr_b16 v[20:21], v8 offset:1024\n" \
  "v_smfmac_f32_16x16x64_bf16 v[72:75], v[32:35], v[208:215], v0\nds_read_b64_tr_b16 v[22:23], v8 offset:1536\nds_read_b64_tr_b16 v[24:25], v8 offset:2048\n" \
  "v_smfmac_f32_16x16x64_bf16 v[72:75], v[36:39], v[216:223], v0\nds_read_b64_tr_b16 v[26:27], v8 offset:2560\nds_read_b64_tr_b16 v[28:29], v8 offset:3072\n" \
  "v_smfmac_f32_16x16x64_bf16 v[72:75], v[40:43], v[224:231], v0\nds_read_b64_tr_b16 v[30:31], v8 offset:3584\nds_read_b64_tr_b16 v[2:3], v2\nv_smfmac_f32_16x16x64_bf16 v[72:75], v[44:47], v[232:239], v0\n" \
  "s_waitcnt lgkmcnt(7)\nv_mfma_f32_16x16x32_bf16 v[56:59], v[16:19], v[16:19], 0\nds_read_b128 v[32:35], v11 offset:4096\nv_mfma_f32_16x16x32_bf16 v[60:63], v[76:79], v[16:19], 0\n" \
  "ds_read_b128 v[36:39], v11 offset:5120\nv_mfma_f32_16x16x32_bf16 v[64:67], v[80:83], v[16:19], 0\nv_smfmac_f32_16x16x64_bf16 v[72:75], v[48:51], v[240:247], v0\n" \
  "s_waitcnt lgkmcnt(7)\nv_mfma_f32_16x16x32_bf16 v[56:59], v[20:23], v[20:23], v[56:59]\nds_read_b128 v[40:43], v11 offset:6144\nds_read_b128 v[44:47], v11 offset:7168\n" \
  "v_mfma_f32_16x16x32_bf16 v[60:63], v[84:87], v[20:23], v[60:63]\nds_read_b128 v[76:79], v11 offset:10240\nv_mfma_f32_16x16x32_bf16 v[64:67], v[88:91], v[20:23], v[64:67]\n" \
  "ds_read_b128 v[80:83], v11 offset:11264\nv_smfmac_f32_16x16x64_bf16 v[72:75], v[52:55], v[248:255], v0\ns_waitcnt lgkmcnt(9)\nv_mfma_f32_16x16x32_bf16 v[56:59], v[24:27], v[24:27], v[56:59]\n" \
  "ds_read_b128 v[84:87], v11 offset:12288\nv_mfma_f32_16x16x32_bf16 v[60:63], v[92:95], v[24:27], v[60:63]\nds_read_b128 v[88:91], v11 offset:13312\nv_mfma_f32_16x16x32_bf16 v[64:67], v[96:99], v[24:27], v[64:67]\n" \
  "s_waitcnt lgkmcnt(9)\nv_mfma_f32_16x16x32_bf16 v[56:59], v[28:31], v[28:31], v[56:59]\nds_read_b128 v[48:51], v11 offset:8192\nds_read_b128 v[52:55], v11 offset:9216\n" \
  "v_mfma_f32_16x16x32_bf16 v[60:63], v[100:103], v[28:31], v[60:63]\nds_read_b128 v[92:95], v11 offset:14336\nv_mfma_f32_16x16x32_bf16 v[64:67], v[104:107], v[28:31], v[64:67]\n" \
  "ds_read_b128 v[96:99], v11 offset:15360\nv_add_f32 v72, v72, v73\nv_add_f32 v74, v74, v75\nv_cvt_pk_bf16_f32 v72, v72, v74\nv_mov_b32 v73, 0\nv_mov_b32 v75, 0\n" \
  "s_cmp_eq_u32 s56, 63\ns_cbranch_scc1 92f\nv_max3_f32 v68, v56, v57, v60\nv_max3_f32 v69, v61, v62, v63\nv_max3_f32 v70, v64, v65, v66\nv_max3_f32 v68, v68, v69, v67\n" \
  "v_max_f32 v68, v68, v70\nv_mov_b32 v69, v68\nds_read_b128 v[100:103], v11 offset:16384\nds_read_b128 v[104:107], v11 offset:17408\nv_permlane16_swap_b32 v68, v69\n" \
  "v_max_f32 v68, v68, v69\nv_mov_b32 v69, v68\nds_read_b128 v[16:19], v11 offset:0\nds_read_b128 v[20:23], v11 offset:1024\nv_permlane32_swap_b32 v68, v69\n" \
  "v_max_f32 v68, v68, v69\nv_mul_f32 v68, s59, v68\nv_pk_fma_f32 v[56:57], v[56:57], s[16:17], v[68:69] op_sel_hi:[1,1,0] neg_lo:[0,0,1] neg_hi:[0,0,1]\n" \
  "v_pk_fma_f32 v[60:61], v[60:61], s[16:17], v[68:69] op_sel_hi:[1,1,0] neg_lo:[0,0,1] neg_hi:[0,0,1]\nv_exp_f32 v56, v56\nv_exp_f32 v57, v57\nv_pk_fma_f32 v[62:63], v[62:63], s[16:17], v[68:69] op_sel_hi:[1,1,0] neg_lo:[0,0,1] neg_hi:[0,0,1]\n" \
  "v_pk_fma_f32 v[64:65], v[64:65], s[16:17], v[68:69] op_sel_hi:[1,1,0] neg_lo:[0,0,1] neg_hi:[0,0,1]\nv_pk_fma_f32 v[66:67], v[66:67], s[16:17], v[68:69] op_sel_hi:[1,1,0] neg_lo:[0,0,1] neg_hi:[0,0,1]\n" \
  "v_cvt_pk_bf16_f32 v74, v56, v57\nv_exp_f32 v60, v60\nv_exp_f32 v61, v61\nv_mfma_f32_16x16x16_bf16 v[252:255], v[12:13], v[74:75], 0\nv_exp_f32 v62, v62\n" \
  "v_exp_f32 v63, v63\nv_mfma_f32_16x16x16_bf16 v[248:251], v[72:73], v[74:75], 0\nv_exp_f32 v64, v64\nv_exp_f32 v65, v65\nv_exp_f32 v66, v66\nv_exp_f32 v67, v67\n" \
  "v_cvt_pk_bf16_f32 v60, v60, v61\nv_cvt_pk_bf16_f32 v61, v62, v63\nv_cvt_pk_bf16_f32 v62, v64, v65\nv_cvt_pk_bf16_f32 v63, v66, v67\nds_read_b128 v[24:27], v11 offset:2048\n" \
  "ds_read_b128 v[28:31], v11 offset:3072\nv_mfma_f32_16x16x32_bf16 v[252:255], v[12:15], v[60:63], v[252:255]\nv_mfma_f32_16x16x32_bf16 v[248:251], v[108:111], v[60:63], v[248:251]\n" \
  "ds_read_b128 v[56:59], v11 offset:18432\nds_read_b128 v[60:63], v11 offset:19456\nv_mov_b64 v[216:217], 0\nv_mov_b64 v[218:219], 0\nv_mov_b64 v[220:221], 0\n" \
  "v_mov_b64 v[222:223], 0\nv_mov_b64 v[224:225], 0\nv_rcp_f32 v68, v252\nv_mov_b64 v[226:227], 0\nv_pk_mul_f32 v[248:249], v[248:249], v[68:69] op_sel_hi:[1,0]\n" \
  "v_pk_mul_f32 v[250:251], v[250:251], v[68:69] op_sel_hi:[1,0]\nv_cvt_pk_bf16_f32 v64, v248, v249\nv_cvt_pk_bf16_f32 v65, v250, v251\nds_write_b64 v9, v[64:65]\n" \
  "v_mov_b64 v[228:229], 0\nv_mov_b64 v[230:231], 0\nv_mov_b64 v[232:233], 0\nv_mov_b64 v[234:235], 0\nv_mov_b64 v[236:237], 0\nv_mov_b64 v[238:239], 0\nv_mov_b64 v[240:241], 0\n" \
  "v_mov_b64 v[242:243], 0\ns_waitcnt lgkmcnt(0)\ns_barrier\nds_read_b128 v[64:67], v10\nv_mov_b64 v[244:245], 0\nv_mov_b64 v[246:247], 0\nv_mov_b64 v[248:249], 0\n" \
  "v_mov_b64 v[250:251], 0\nv_mov_b64 v[252:253], 0\nv_mov_b64 v[254:255], 0\ns_waitcnt lgkmcnt(0)\nv_smfmac_f32_16x16x64_bf16 v[216:219], v[64:67], v[16:23], v0\n" \
  "v_smfmac_f32_16x16x64_bf16 v[220:223], v[64:67], v[24:31], v0\ns_nop 5\nv_smfmac_f32_16x16x64_bf16 v[224:227], v[64:67], v[32:39], v0\nv_add_f32 v216, v216, v217\n" \
  "v_add_f32 v218, v218, v219\nv_cvt_pk_bf16_f32 v16, v216, v218\nv_bfi_b32 v16, 1, s57, v16\nv_smfmac_f32_16x16x64_bf16 v[228:231], v[64:67], v[40:47], v0\n" \
  "v_add_f32 v220, v220, v221\nv_add_f32 v222, v222, v223\nv_cvt_pk_bf16_f32 v17, v220, v222\nv_smfmac_f32_16x16x64_bf16 v[232:235], v[64:67], v[48:55], v0\n" \
  "v_add_f32 v224, v224, v225\nv_add_f32 v226, v226, v227\nv_cvt_pk_bf16_f32 v18, v224, v226\nv_bfi_b32 v18, 1, s57, v18\nv_smfmac_f32_16x16x64_bf16 v[236:239], v[64:67], v[76:83], v0\n" \
  "v_add_f32 v228, v228, v229\nv_add_f32 v230, v230, v231\nv_cvt_pk_bf16_f32 v19, v228, v230\nglobal_store_dwordx4 v5, v[16:19], s[44:45] sc1\nv_smfmac_f32_16x16x64_bf16 v[240:243], v[64:67], v[84:91], v0\n" \
  "v_add_f32 v232, v232, v233\nv_add_f32 v234, v234, v235\nv_cvt_pk_bf16_f32 v20, v232, v234\nv_bfi_b32 v20, 1, s57, v20\nv_smfmac_f32_16x16x64_bf16 v[244:247], v[64:67], v[92:99], v0\n" \
  "v_add_f32 v236, v236, v237\nv_add_f32 v238, v238, v239\nv_cvt_pk_bf16_f32 v21, v236, v238\nv_smfmac_f32_16x16x64_bf16 v[248:251], v[64:67], v[100:107], v0\n" \
  "v_add_f32 v240, v240, v241\nv_add_f32 v242, v242, v243\nv_cvt_pk_bf16_f32 v22, v240, v242\nv_bfi_b32 v22, 1, s57, v22\nv_smfmac_f32_16x16x64_bf16 v[252:255], v[64:67], v[56:63], v0\n" \
  "v_add_f32 v244, v244, v245\nv_add_f32 v246, v246, v247\nv_cvt_pk_bf16_f32 v23, v244, v246\nglobal_store_dwordx4 v5, v[20:23], s[46:47] sc1\ns_nop 0\nv_add_f32 v248, v248, v249\n" \
  "v_add_f32 v250, v250, v251\nv_cvt_pk_bf16_f32 v24, v248, v250\nv_bfi_b32 v24, 1, s57, v24\nv_add_f32 v252, v252, v253\nv_add_f32 v254, v254, v255\nv_cvt_pk_bf16_f32 v25, v252, v254\n" \
  "global_store_dwordx2 v6, v[24:25], s[44:45] sc1\ns_lshl_b32 s62, s36, 11\ns_sub_u32 s62, s87, s62\nv_add_u32 v38, s62, v1\nv_add_u32 v39, s88, v1\nds_read_b128 v[76:79], v38 offset:0\n" \
  "ds_read_b128 v[80:83], v38 offset:1024\nds_read_b128 v[84:87], v38 offset:2048\nds_read_b128 v[88:91], v38 offset:3072\nds_read_b128 v[92:95], v38 offset:4096\n" \
  "ds_read_b128 v[96:99], v38 offset:5120\nds_read_b128 v[100:103], v38 offset:6144\nds_read_b128 v[104:107], v38 offset:7168\nds_read_b128 v[108:111], v39\n" \
  "s_waitcnt lgkmcnt(4)\nv_mov_b32 v44, v72\ns_add_u32 s56, s56, 1\ns_lshr_b32 s57, s56, 1\ns_xor_b32 s57, s57, 1\ns_mov_b64 s[62:63], s[44:45]\ns_mov_b64 s[44:45], s[76:77]\n" \
  "s_mov_b64 s[76:77], s[62:63]\ns_add_u32 s46, s44, 0x20000\ns_addc_u32 s47, s45, 0\ns_add_u32 s40, s40, 16384\ns_addc_u32 s41, s41, 0\ns_lshr_b32 s62, s56, 2\n" \
  "s_lshl_b32 s62, s62, 14\ns_and_b32 s63, s56, 3\ns_lshl_b32 s63, s63, 11\ns_add_u32 s62, s62, s63\ns_add_u32 s42, s68, s62\ns_addc_u32 s43, s69, 0\nds_read_b128 v[216:219], v11 offset:20480\n" \
  "ds_read_b128 v[220:223], v11 offset:21504\nds_read_b128 v[224:227], v11 offset:22528\nds_read_b128 v[228:231], v11 offset:23552\nds_read_b128 v[232:235], v11 offset:24576\n" \
  "ds_read_b128 v[236:239], v11 offset:25600\nds_read_b128 v[240:243], v11 offset:26624\nds_read_b128 v[244:247], v11 offset:27648\nds_read_b128 v[248:251], v11 offset:28672\n" \
  "ds_read_b128 v[252:255], v11 offset:29696\nv_mov_b64 v[56:57], 0\nv_mov_b64 v[58:59], 0\nv_mov_b64 v[60:61], 0\nv_mov_b64 v[62:63], 0\nv_mov_b64 v[64:65], 0\n" \
  "v_mov_b64 v[66:67], 0\nv_mov_b64 v[68:69], 0\nv_mov_b64 v[70:71], 0\nv_mov_b64 v[72:73], 0\nv_mov_b64 v[74:75], 0\ns_lshl_b32 s62, s36, 11\nv_lshlrev_b32 v43, 3, v1\n" \
  "v_add_u32 v43, s62, v43\nv_mov_b32 v46, s14\nv_writelane_b32 v46, s15, 1\nv_writelane_b32 v46, s94, 2\nv_mov_b32 v47, 64\ns_mov_b64 exec, 0xffff\nglobal_load_dword v41, v43, s[30:31] sc1\n" \
  "s_mov_b64 exec, 7\nglobal_load_dword v47, v46, s[12:13] sc1\ns_mov_b64 exec, -1\nv_lshrrev_b32 v45, 1, v1\nglobal_store_dwordx2 v45, v[2:3], s[52:53] sc1\n" \
  "v_lshrrev_b32 v45, 2, v1\nglobal_store_dword v45, v44, s[54:55] sc1\ns_add_u32 s52, s52, 262144\ns_addc_u32 s53, s53, 0\ns_lshr_b32 s35, s52, 8\ns_andn2_b32 s52, s52, 0x2000\n" \
  "s_and_b32 s35, s35, 0x2000\ns_xor_b32 s35, s35, s95\ns_xor_b32 s52, s52, s35\ns_add_u32 s54, s54, 131072\ns_addc_u32 s55, s55, 0\ns_lshr_b32 s35, s54, 8\n" \
  "s_andn2_b32 s54, s54, 0x2000\ns_and_b32 s35, s35, 0x2000\ns_xor_b32 s35, s35, s95\ns_xor_b32 s54, s54, s35\ns_add_u32 s48, s48, 524288\ns_addc_u32 s49, s49, 0\n" \
  "s_lshr_b32 s35, s48, 8\ns_or_b32 s48, s48, 0x2000\ns_and_b32 s35, s35, 0x2000\ns_xor_b32 s35, s35, s95\ns_xor_b32 s48, s48, s35\ns_add_u32 s50, s50, 524288\n" \
  "s_addc_u32 s51, s51, 0\ns_lshr_b32 s35, s50, 8\ns_or_b32 s50, s50, 0x2000\ns_and_b32 s35, s35, 0x2000\ns_xor_b32 s35, s35, s95\ns_xor_b32 s50, s50, s35\n" \
  "s_mov_b32 s58, 0x200000\ns_branch 10b\n92:\ns_waitcnt lgkmcnt(0)\nv_lshrrev_b32 v45, 1, v1\nglobal_store_dwordx2 v45, v[2:3], s[52:53] sc1\nv_lshrrev_b32 v45, 2, v1\n" \
  "global_store_dword v45, v72, s[54:55] sc1\ns_waitcnt vmcnt(0)\nv_mov_b32 v45, 64\ns_mov_b64 exec, 1\nglobal_store_dword v1, v45, s[10:11] sc1\ns_mov_b64 exec, -1\n" \
  "s_branch 91f\n90:\nv_mov_b32 v40, 0\nv_mov_b32 v41, 1\nglobal_store_dword v40, v41, s[18:19] sc1\n91:\ns_waitcnt vmcnt(0) lgkmcnt(0)\ns_endpgm\n93:\ns_waitcnt vmcnt(0) lgkmcnt(0)\n"
#define WORKER_EARLY_ASM \
  "s_mov_b32 s36, %[wave]\ns_mov_b64 s[20:21], %[wq]\ns_mov_b64 s[22:23], %[wk]\ns_mov_b64 s[24:25], %[wv]\ns_mov_b64 s[26:27], %[wo]\nv_mov_b32 v108, 0\n" \
  "global_load_dwordx2 v[108:109], v108, %[ep] sc1\ns_mul_i32 s66, s36, 10240\ns_add_u32 s62, s26, s66\ns_addc_u32 s63, s27, 0\ns_add_u32 s62, s62, 0\ns_addc_u32 s63, s63, 0\n" \
  "s_add_u32 s66, s66, 40960\ns_mov_b32 m0, s66\ns_add_u32 s66, s66, 4096\nglobal_load_lds_dwordx4 %[l16], s[62:63] offset:0\nglobal_load_lds_dwordx4 %[l16], s[62:63] offset:1024\n" \
  "global_load_lds_dwordx4 %[l16], s[62:63] offset:2048\nglobal_load_lds_dwordx4 %[l16], s[62:63] offset:3072\ns_add_u32 s62, s62, 4096\ns_addc_u32 s63, s63, 0\n" \
  "s_mov_b32 m0, s66\ns_add_u32 s66, s66, 4096\nglobal_load_lds_dwordx4 %[l16], s[62:63] offset:0\nglobal_load_lds_dwordx4 %[l16], s[62:63] offset:1024\nglobal_load_lds_dwordx4 %[l16], s[62:63] offset:2048\n" \
  "global_load_lds_dwordx4 %[l16], s[62:63] offset:3072\ns_add_u32 s62, s62, 4096\ns_addc_u32 s63, s63, 0\ns_mov_b32 m0, s66\ns_add_u32 s66, s66, 4096\nglobal_load_lds_dwordx4 %[l16], s[62:63] offset:0\n" \
  "global_load_lds_dwordx4 %[l16], s[62:63] offset:1024\ns_mul_i32 s66, s36, 10240\ns_add_u32 s62, s26, s66\ns_addc_u32 s63, s27, 0\ns_add_u32 s62, s62, 40960\n" \
  "s_addc_u32 s63, s63, 0\ns_add_u32 s66, s66, 81920\ns_mov_b32 m0, s66\ns_add_u32 s66, s66, 4096\nglobal_load_lds_dwordx4 %[l16], s[62:63] offset:0\nglobal_load_lds_dwordx4 %[l16], s[62:63] offset:1024\n" \
  "global_load_lds_dwordx4 %[l16], s[62:63] offset:2048\nglobal_load_lds_dwordx4 %[l16], s[62:63] offset:3072\ns_add_u32 s62, s62, 4096\ns_addc_u32 s63, s63, 0\n" \
  "s_mov_b32 m0, s66\ns_add_u32 s66, s66, 4096\nglobal_load_lds_dwordx4 %[l16], s[62:63] offset:0\nglobal_load_lds_dwordx4 %[l16], s[62:63] offset:1024\nglobal_load_lds_dwordx4 %[l16], s[62:63] offset:2048\n" \
  "global_load_lds_dwordx4 %[l16], s[62:63] offset:3072\ns_add_u32 s62, s62, 4096\ns_addc_u32 s63, s63, 0\ns_mov_b32 m0, s66\ns_add_u32 s66, s66, 4096\nglobal_load_lds_dwordx4 %[l16], s[62:63] offset:0\n" \
  "global_load_lds_dwordx4 %[l16], s[62:63] offset:1024\ns_mov_b32 s67, 0\ns_add_u32 s62, s20, s67\ns_addc_u32 s63, s21, 0\ns_lshl_b32 s66, s36, 12\ns_add_u32 s66, s66, 122880\n" \
  "s_mov_b32 m0, s66\ns_add_u32 s66, s66, 1024\nglobal_load_lds_dwordx4 %[qs], s[62:63]\ns_add_u32 s62, s62, 32768\ns_addc_u32 s63, s63, 0\ns_mov_b32 m0, s66\n" \
  "s_add_u32 s66, s66, 1024\nglobal_load_lds_dwordx4 %[qs], s[62:63]\ns_add_u32 s62, s62, 32768\ns_addc_u32 s63, s63, 0\ns_mov_b32 m0, s66\ns_add_u32 s66, s66, 1024\n" \
  "global_load_lds_dwordx4 %[qs], s[62:63]\ns_add_u32 s62, s62, 32768\ns_addc_u32 s63, s63, 0\ns_mov_b32 m0, s66\ns_add_u32 s66, s66, 1024\nglobal_load_lds_dwordx4 %[qs], s[62:63]\n" \
  "s_add_u32 s62, s22, s67\ns_addc_u32 s63, s23, 0\ns_lshl_b32 s66, s36, 12\ns_add_u32 s66, s66, 139264\ns_mov_b32 m0, s66\ns_add_u32 s66, s66, 1024\nglobal_load_lds_dwordx4 %[qs], s[62:63]\n" \
  "s_add_u32 s62, s62, 32768\ns_addc_u32 s63, s63, 0\ns_mov_b32 m0, s66\ns_add_u32 s66, s66, 1024\nglobal_load_lds_dwordx4 %[qs], s[62:63]\ns_add_u32 s62, s62, 32768\n" \
  "s_addc_u32 s63, s63, 0\ns_mov_b32 m0, s66\ns_add_u32 s66, s66, 1024\nglobal_load_lds_dwordx4 %[qs], s[62:63]\ns_add_u32 s62, s62, 32768\ns_addc_u32 s63, s63, 0\n" \
  "s_mov_b32 m0, s66\ns_add_u32 s66, s66, 1024\nglobal_load_lds_dwordx4 %[qs], s[62:63]\ns_add_u32 s62, s24, s67\ns_addc_u32 s63, s25, 0\ns_lshl_b32 s66, s36, 11\n" \
  "s_add_u32 s66, s66, 155648\ns_mov_b32 m0, s66\ns_add_u32 s66, s66, 1024\nglobal_load_lds_dwordx4 %[vs], s[62:63]\ns_add_u32 s62, s62, 65536\ns_addc_u32 s63, s63, 0\n" \
  "s_mov_b32 m0, s66\ns_add_u32 s66, s66, 1024\nglobal_load_lds_dwordx4 %[vs], s[62:63]\n"
#define WORKER_CLOB \
  "v0","v1","v2","v3","v4","v5","v6","v7","v8","v9","v10","v11","v12","v13","v14","v15","v16","v17","v18","v19","v20","v21","v22","v23","v24","v25","v26", \
  "v27","v28","v29","v30","v31","v32","v33","v34","v35","v36","v37","v38","v39","v40","v41","v42","v43","v44","v45","v46","v47","v48","v49","v50","v51","v52", \
  "v53","v54","v55","v56","v57","v58","v59","v60","v61","v62","v63","v64","v65","v66","v67","v68","v69","v70","v71","v72","v73","v74","v75","v76","v77","v78", \
  "v79","v80","v81","v82","v83","v84","v85","v86","v87","v88","v89","v90","v91","v92","v93","v94","v95","v96","v97","v98","v99","v100","v101","v102","v103", \
  "v104","v105","v106","v107","v110","v111","v112","v113","v114","v115","v116","v117","v118","v119","v120","v121","v122","v123","v124","v125","v126","v127", \
  "v128","v129","v130","v131","v132","v133","v134","v135","v136","v137","v138","v139","v140","v141","v142","v143","v144","v145","v146","v147","v148","v149", \
  "v150","v151","v152","v153","v154","v155","v156","v157","v158","v159","v160","v161","v162","v163","v164","v165","v166","v167","v168","v169","v170","v171", \
  "v172","v173","v174","v175","v176","v177","v178","v179","v180","v181","v182","v183","v184","v185","v186","v187","v188","v189","v190","v191","v192","v193", \
  "v194","v195","v196","v197","v198","v199","v200","v201","v202","v203","v204","v205","v206","v207","v208","v209","v210","v211","v212","v213","v214","v215", \
  "v216","v217","v218","v219","v220","v221","v222","v223","v224","v225","v226","v227","v228","v229","v230","v231","v232","v233","v234","v235","v236","v237", \
  "v238","v239","v240","v241","v242","v243","v244","v245","v246","v247","v248","v249","v254","v255","a0","a1","a2","a3","a4","a5","a6","a7","a8","a9","a10", \
  "a11","a12","a13","a14","a15","a16","a17","a18","a19","a20","a21","a22","a23","a24","a25","a26","a27","a28","a29","a30","a31","a32","a33","a34","a35","a36", \
  "a37","a38","a39","a40","a41","a42","a43","a44","a45","a46","a47","a48","a49","a50","a51","a52","a53","a54","a55","a56","a57","a58","a59","a60","a61","a62", \
  "a63","a64","a65","a66","a67","a68","a69","a70","a71","a72","a73","a74","a75","a76","a77","a78","a79","a80","a81","a82","a83","a84","a85","a86","a87","a88", \
  "a89","a90","a91","a92","a93","a94","a95","a96","a97","a98","a99","a100","a101","a102","a103","a104","a105","a106","a107","a108","a109","a110","a111","a112", \
  "a113","a114","a115","a116","a117","a118","a119","a120","a121","a122","a123","a124","a125","a126","a127","a128","a129","a130","a131","a132","a133","a134", \
  "a135","a136","a137","a138","a139","a140","a141","a142","a143","a144","a145","a146","a147","a148","a149","a150","a151","a152","a153","a154","a155","a156", \
  "a157","a158","a159","a160","a161","a162","a163","a164","a165","a166","a167","a168","a169","a170","a171","a172","a173","a174","a175","a176","a177","a178", \
  "a179","a180","a181","a182","a183","a184","a185","a186","a187","a188","a189","a190","a191","a192","a193","a194","a195","a196","a197","a198","a199","a200", \
  "a201","a202","a203","a204","a205","a206","a207","a208","a209","a210","a211","a212","a213","a214","a215","a216","a217","a218","a219","a220","a221","a222", \
  "a223","a224","a225","a226","a227","a228","a229","a230","a231","a232","a233","a234","a235","a236","a237","a238","a239","a240","a241","a242","a243","a244", \
  "a245","a246","a247","a248","a249","a250","a251","a252","a253","a254","a255","s10","s11","s12","s13","s14","s15","s16","s17","s18","s19","s20","s21","s22", \
  "s23","s24","s25","s26","s27","s28","s29","s30","s31","s34","s35","s36","s37","s38","s39","s40","s41","s42","s43","s44","s45","s46","s47","s48","s49","s50", \
  "s51","s52","s53","s54","s55","s56","s57","s58","s59","s60","s61","s62","s63","s64","s65","s66","s67","s68","s69","s70","s71","s72","s73","s74","s75","s76", \
  "s77","s78","s79","s80","s81","s82","s83","s84","s85","s86","s87","s88","s89","s90","s91","s92","s93","s94","s95"
#define REDUCER_ASM \
  "s_mov_b64 s[40:41], %[mo]\ns_mov_b32 s44, 1\ns_mov_b32 s45, -1\ns_mov_b32 s51, 0\ns_mov_b64 s[64:65], %[pl]\ns_mov_b64 s[66:67], %[pl2]\ns_mov_b32 s47, %[tr]\n" \
  "s_add_u32 s58, s47, 40\ns_mov_b32 s70, 0\ns_mov_b32 s71, 0\ns_branch 10f\n15:\ns_cmp_eq_u32 s68, s44\ns_cbranch_scc1 11f\ns_mov_b32 s68, s44\ns_add_u32 s69, s47, 12\n" \
  "s_mov_b32 s71, 1\ns_branch 30f\n10:\ns_mov_b32 s46, 0x200000\ns_add_u32 s45, s44, -1\ns_bfe_i32 s45, s45, 0x10001\ns_not_b32 s45, s45\ns_lshl_b32 s48, s44, 14\n" \
  "s_lshr_b32 s49, s44, 2\ns_lshl_b32 s49, s49, 14\ns_and_b32 s50, s44, 3\ns_lshl_b32 s50, s50, 11\ns_add_u32 s49, s49, s50\ns_cmp_eq_u32 %[isb], 0\ns_cselect_b32 s48, s48, s49\n" \
  "s_add_u32 s42, s40, s48\ns_addc_u32 s43, s41, 0\ns_add_u32 s68, s44, -1\ns_add_u32 s69, s47, %[tw]\ns_mul_i32 s71, s71, 16\ns_add_u32 s69, s69, s71\ns_cmp_eq_u32 s44, 1\n" \
  "s_cselect_b32 s68, s44, s68\ns_cselect_b32 s69, s47, s69\ns_cselect_b32 s71, 1, 0\ns_sleep 8\n30:\nglobal_load_dword v61, %[fl], %[tli] sc1\ns_memrealtime s[60:61]\n" \
  "s_waitcnt vmcnt(0) lgkmcnt(0)\nv_and_b32 v60, 127, v61\ns_lshl_b32 s61, s60, 7\nv_cmp_gt_u32 vcc, s68, v60\ns_cbranch_vccz 31f\ns_sleep 3\ns_sub_u32 s46, s46, 1\n" \
  "s_cbranch_scc0 30b\ns_branch 90f\n31:\nv_cmp_lt_u32 vcc, s68, v60\ns_cbranch_vccz 13f\ns_mov_b32 s68, s44\ns_mov_b32 s69, s47\ns_branch 30b\n13:\nv_subrev_u32 v60, s61, v61\n" \
  "s_nop 1\nv_max_i32_dpp v60, v60, v60 row_ror:1 row_mask:0xf bank_mask:0xf\ns_nop 1\nv_max_i32_dpp v60, v60, v60 row_ror:2 row_mask:0xf bank_mask:0xf\ns_nop 1\n" \
  "v_max_i32_dpp v60, v60, v60 row_ror:4 row_mask:0xf bank_mask:0xf\ns_nop 1\nv_max_i32_dpp v60, v60, v60 row_ror:8 row_mask:0xf bank_mask:0xf\nv_mov_b32 v61, v60\n" \
  "s_nop 1\nv_permlane16_swap_b32 v60, v61\nv_max_i32 v60, v60, v61\nv_mov_b32 v61, v60\ns_nop 1\nv_permlane32_swap_b32 v60, v61\nv_max_i32 v60, v60, v61\n" \
  "s_nop 0\nv_readfirstlane_b32 s62, v60\ns_nop 3\ns_ashr_i32 s62, s62, 7\ns_add_u32 s62, s62, s60\ns_add_u32 s62, s62, s69\ns_cmp_eq_u32 s71, 1\ns_cbranch_scc1 12f\n" \
  "s_sleep 10\nglobal_store_dwordx4 %[l16], v[16:19], s[66:67] offset:-4096 sc1\nglobal_store_dwordx4 %[l16], v[20:23], s[66:67] offset:-3072 sc1\nglobal_store_dwordx4 %[l16], v[24:27], s[66:67] offset:-2048 sc1\n" \
  "global_store_dwordx4 %[l16], v[28:31], s[66:67] offset:-1024 sc1\nglobal_store_dwordx4 %[l16], v[32:35], s[66:67] offset:0 sc1\nglobal_store_dwordx4 %[l16], v[36:39], s[66:67] offset:1024 sc1\n" \
  "global_store_dwordx4 %[l16], v[40:43], s[66:67] offset:2048 sc1\nglobal_store_dwordx4 %[l16], v[44:47], s[66:67] offset:3072 sc1\n12:\n32:\ns_memrealtime s[60:61]\n" \
  "s_waitcnt lgkmcnt(0)\ns_sub_u32 s63, s60, s62\ns_cmp_ge_i32 s63, 0\ns_cbranch_scc1 33f\ns_memrealtime s[60:61]\ns_waitcnt lgkmcnt(0)\ns_sub_u32 s63, s60, s62\n" \
  "s_cmp_ge_i32 s63, 0\ns_cbranch_scc1 33f\ns_memrealtime s[60:61]\ns_waitcnt lgkmcnt(0)\ns_sub_u32 s63, s60, s62\ns_cmp_ge_i32 s63, 0\ns_cbranch_scc1 33f\n" \
  "s_memrealtime s[60:61]\ns_waitcnt lgkmcnt(0)\ns_sub_u32 s63, s60, s62\ns_cmp_ge_i32 s63, 0\ns_cbranch_scc1 33f\ns_branch 32b\n33:\ns_sub_u32 s59, s46, 1\n" \
  "11:\nglobal_load_dwordx4 v[16:19], %[l16], s[64:65] offset:-4096 sc1\nglobal_load_dwordx4 v[20:23], %[l16], s[64:65] offset:-3072 sc1\nglobal_load_dwordx4 v[24:27], %[l16], s[64:65] offset:-2048 sc1\n" \
  "global_load_dwordx4 v[28:31], %[l16], s[64:65] offset:-1024 sc1\nglobal_load_dwordx4 v[32:35], %[l16], s[64:65] offset:0 sc1\nglobal_load_dwordx4 v[36:39], %[l16], s[64:65] offset:1024 sc1\n" \
  "global_load_dwordx4 v[40:43], %[l16], s[64:65] offset:2048 sc1\nglobal_load_dwordx4 v[44:47], %[l16], s[64:65] offset:3072 sc1\ns_sub_u32 s46, s46, 1\n" \
  "s_cbranch_scc1 90f\ns_waitcnt vmcnt(7)\nv_mfma_f32_16x16x32_bf16 v[48:51], %[sel], v[16:19], 0\nv_bitop3_b32 v52, v16, v18, s45 bitop3:0x7e\ns_waitcnt vmcnt(6)\n" \
  "v_mfma_f32_16x16x32_bf16 v[48:51], %[sel], v[20:23], v[48:51]\nv_bitop3_b32 v53, v20, v22, s45 bitop3:0x7e\ns_waitcnt vmcnt(5)\nv_mfma_f32_16x16x32_bf16 v[48:51], %[sel], v[24:27], v[48:51]\n" \
  "v_bitop3_b32 v54, v24, v26, s45 bitop3:0x7e\nv_or3_b32 v52, v52, v53, v54\ns_waitcnt vmcnt(4)\nv_mfma_f32_16x16x32_bf16 v[48:51], %[sel], v[28:31], v[48:51]\n" \
  "v_bitop3_b32 v55, v28, v30, s45 bitop3:0x7e\ns_waitcnt vmcnt(3)\nv_mfma_f32_16x16x32_bf16 v[48:51], %[sel], v[32:35], v[48:51]\nv_bitop3_b32 v56, v32, v34, s45 bitop3:0x7e\n" \
  "v_or3_b32 v52, v52, v55, v56\ns_waitcnt vmcnt(2)\nv_mfma_f32_16x16x32_bf16 v[48:51], %[sel], v[36:39], v[48:51]\nv_bitop3_b32 v57, v36, v38, s45 bitop3:0x7e\n" \
  "s_waitcnt vmcnt(1)\nv_mfma_f32_16x16x32_bf16 v[48:51], %[sel], v[40:43], v[48:51]\nv_bitop3_b32 v58, v40, v42, s45 bitop3:0x7e\nv_or3_b32 v52, v52, v57, v58\n" \
  "s_waitcnt vmcnt(0)\nv_mfma_f32_16x16x32_bf16 v[48:51], %[sel], v[44:47], v[48:51]\nv_bitop3_b32 v59, v44, v46, s45 bitop3:0x7e\nv_or_b32 v52, v52, v59\n" \
  "v_and_b32 v52, 1, v52\nv_cmp_ne_u32 vcc, 0, v52\ns_cbranch_vccnz 15b\ns_nop 3\nv_add_f32_dpp v52, v48, v48 row_ror:8 row_mask:0xf bank_mask:0xf\nv_add_f32_dpp v53, v49, v49 row_ror:8 row_mask:0xf bank_mask:0xf\n" \
  "v_add_f32_dpp v54, v50, v50 row_ror:8 row_mask:0xf bank_mask:0xf\nv_add_f32_dpp v55, v51, v51 row_ror:8 row_mask:0xf bank_mask:0xf\nv_cvt_pk_bf16_f32 v56, v52, v53\n" \
  "v_cvt_pk_bf16_f32 v57, v54, v55\ns_mov_b64 exec, 0x00ff00ff\nv_min_u32 v56, -2, v56\nglobal_store_dwordx2 %[oaddr], v[56:57], s[42:43] sc1\ns_mov_b64 exec, -1\n" \
  "s_mov_b64 s[48:49], s[64:65]\ns_mov_b64 s[64:65], s[66:67]\ns_mov_b64 s[66:67], s[48:49]\ns_add_u32 s44, s44, 1\ns_cmp_le_u32 s44, 63\ns_cbranch_scc1 10b\n" \
  "s_branch 91f\n90:\nv_mov_b32 v52, 0\nv_mov_b32 v53, 2\nglobal_store_dword v52, v53, %[err] sc1\n91:\ns_waitcnt vmcnt(0)\n"
#define REDUCER_CLOB \
  "v16","v17","v18","v19","v20","v21","v22","v23","v24","v25","v26","v27","v28","v29","v30","v31","v32","v33","v34","v35","v36","v37","v38","v39","v40","v41", \
  "v42","v43","v44","v45","v46","v47","v48","v49","v50","v51","v52","v53","v54","v55","v56","v57","v58","v59","v60","v61","v62","v63","s40","s41","s42","s43", \
  "s44","s45","s46","s47","s48","s49","s50","s51","s52","s53","s54","s55","s56","s57","s58","s59","s60","s61","s62","s63","s64","s65","s66","s67","s68","s69", \
  "s70","s71"
#define R1_LOAD_ASM \
  "v_mov_b32 v198, 0\nglobal_load_dwordx2 v[198:199], v198, %[ep] sc1\ns_mul_i32 s92, %[rs], 0\ns_mov_b64 s[90:91], %[w]\ns_add_u32 s90, s90, s92\ns_addc_u32 s91, s91, 0\n" \
  "s_lshl_b32 s93, %[wave], 13\ns_add_u32 s93, s93, 0\ns_lshl_b32 s92, %[rs], 1\nv_xor_b32 v192, 0, %[vc]\nv_min_u32 v192, %[lim], v192\ns_mov_b32 m0, s93\n" \
  "v_add_u32 v192, v192, %[vrow]\ns_add_u32 s93, s93, 1024\nglobal_load_lds_dwordx4 v192, s[90:91]\ns_add_u32 s90, s90, s92\ns_addc_u32 s91, s91, 0\nv_xor_b32 v192, 64, %[vc]\n" \
  "v_min_u32 v192, %[lim], v192\ns_mov_b32 m0, s93\nv_add_u32 v192, v192, %[vrow]\ns_add_u32 s93, s93, 1024\nglobal_load_lds_dwordx4 v192, s[90:91]\ns_add_u32 s90, s90, s92\n" \
  "s_addc_u32 s91, s91, 0\nv_xor_b32 v192, 0, %[vc]\nv_min_u32 v192, %[lim], v192\ns_mov_b32 m0, s93\nv_add_u32 v192, v192, %[vrow]\ns_add_u32 s93, s93, 1024\n" \
  "global_load_lds_dwordx4 v192, s[90:91]\ns_add_u32 s90, s90, s92\ns_addc_u32 s91, s91, 0\nv_xor_b32 v192, 64, %[vc]\nv_min_u32 v192, %[lim], v192\ns_mov_b32 m0, s93\n" \
  "v_add_u32 v192, v192, %[vrow]\ns_add_u32 s93, s93, 1024\nglobal_load_lds_dwordx4 v192, s[90:91]\ns_add_u32 s90, s90, s92\ns_addc_u32 s91, s91, 0\nv_xor_b32 v192, 128, %[vc]\n" \
  "v_min_u32 v192, %[lim], v192\ns_mov_b32 m0, s93\nv_add_u32 v192, v192, %[vrow]\ns_add_u32 s93, s93, 1024\nglobal_load_lds_dwordx4 v192, s[90:91]\ns_add_u32 s90, s90, s92\n" \
  "s_addc_u32 s91, s91, 0\nv_xor_b32 v192, 192, %[vc]\nv_min_u32 v192, %[lim], v192\ns_mov_b32 m0, s93\nv_add_u32 v192, v192, %[vrow]\ns_add_u32 s93, s93, 1024\n" \
  "global_load_lds_dwordx4 v192, s[90:91]\ns_add_u32 s90, s90, s92\ns_addc_u32 s91, s91, 0\nv_xor_b32 v192, 128, %[vc]\nv_min_u32 v192, %[lim], v192\ns_mov_b32 m0, s93\n" \
  "v_add_u32 v192, v192, %[vrow]\ns_add_u32 s93, s93, 1024\nglobal_load_lds_dwordx4 v192, s[90:91]\ns_add_u32 s90, s90, s92\ns_addc_u32 s91, s91, 0\nv_xor_b32 v192, 192, %[vc]\n" \
  "v_min_u32 v192, %[lim], v192\ns_mov_b32 m0, s93\nv_add_u32 v192, v192, %[vrow]\ns_add_u32 s93, s93, 1024\nglobal_load_lds_dwordx4 v192, s[90:91]\ns_mul_i32 s92, %[rs], 64\n" \
  "s_mov_b64 s[90:91], %[w]\ns_add_u32 s90, s90, s92\ns_addc_u32 s91, s91, 0\ns_lshl_b32 s93, %[wave], 13\ns_add_u32 s93, s93, 32768\ns_lshl_b32 s92, %[rs], 1\n" \
  "v_xor_b32 v192, 0, %[vc]\nv_min_u32 v192, %[lim], v192\ns_mov_b32 m0, s93\nv_add_u32 v192, v192, %[vrow]\ns_add_u32 s93, s93, 1024\nglobal_load_lds_dwordx4 v192, s[90:91]\n" \
  "s_add_u32 s90, s90, s92\ns_addc_u32 s91, s91, 0\nv_xor_b32 v192, 64, %[vc]\nv_min_u32 v192, %[lim], v192\ns_mov_b32 m0, s93\nv_add_u32 v192, v192, %[vrow]\n" \
  "s_add_u32 s93, s93, 1024\nglobal_load_lds_dwordx4 v192, s[90:91]\ns_add_u32 s90, s90, s92\ns_addc_u32 s91, s91, 0\nv_xor_b32 v192, 0, %[vc]\nv_min_u32 v192, %[lim], v192\n" \
  "s_mov_b32 m0, s93\nv_add_u32 v192, v192, %[vrow]\ns_add_u32 s93, s93, 1024\nglobal_load_lds_dwordx4 v192, s[90:91]\ns_add_u32 s90, s90, s92\ns_addc_u32 s91, s91, 0\n" \
  "v_xor_b32 v192, 64, %[vc]\nv_min_u32 v192, %[lim], v192\ns_mov_b32 m0, s93\nv_add_u32 v192, v192, %[vrow]\ns_add_u32 s93, s93, 1024\nglobal_load_lds_dwordx4 v192, s[90:91]\n" \
  "s_add_u32 s90, s90, s92\ns_addc_u32 s91, s91, 0\nv_xor_b32 v192, 128, %[vc]\nv_min_u32 v192, %[lim], v192\ns_mov_b32 m0, s93\nv_add_u32 v192, v192, %[vrow]\n" \
  "s_add_u32 s93, s93, 1024\nglobal_load_lds_dwordx4 v192, s[90:91]\ns_add_u32 s90, s90, s92\ns_addc_u32 s91, s91, 0\nv_xor_b32 v192, 192, %[vc]\nv_min_u32 v192, %[lim], v192\n" \
  "s_mov_b32 m0, s93\nv_add_u32 v192, v192, %[vrow]\ns_add_u32 s93, s93, 1024\nglobal_load_lds_dwordx4 v192, s[90:91]\ns_add_u32 s90, s90, s92\ns_addc_u32 s91, s91, 0\n" \
  "v_xor_b32 v192, 128, %[vc]\nv_min_u32 v192, %[lim], v192\ns_mov_b32 m0, s93\nv_add_u32 v192, v192, %[vrow]\ns_add_u32 s93, s93, 1024\nglobal_load_lds_dwordx4 v192, s[90:91]\n" \
  "s_add_u32 s90, s90, s92\ns_addc_u32 s91, s91, 0\nv_xor_b32 v192, 192, %[vc]\nv_min_u32 v192, %[lim], v192\ns_mov_b32 m0, s93\nv_add_u32 v192, v192, %[vrow]\n" \
  "s_add_u32 s93, s93, 1024\nglobal_load_lds_dwordx4 v192, s[90:91]\ns_mul_i32 s92, %[rs], 128\ns_mov_b64 s[90:91], %[w]\ns_add_u32 s90, s90, s92\ns_addc_u32 s91, s91, 0\n" \
  "s_lshl_b32 s93, %[wave], 13\ns_add_u32 s93, s93, 65536\ns_lshl_b32 s92, %[rs], 1\nv_xor_b32 v192, 0, %[vc]\nv_min_u32 v192, %[lim], v192\ns_mov_b32 m0, s93\n" \
  "v_add_u32 v192, v192, %[vrow]\ns_add_u32 s93, s93, 1024\nglobal_load_lds_dwordx4 v192, s[90:91]\ns_add_u32 s90, s90, s92\ns_addc_u32 s91, s91, 0\nv_xor_b32 v192, 64, %[vc]\n" \
  "v_min_u32 v192, %[lim], v192\ns_mov_b32 m0, s93\nv_add_u32 v192, v192, %[vrow]\ns_add_u32 s93, s93, 1024\nglobal_load_lds_dwordx4 v192, s[90:91]\ns_add_u32 s90, s90, s92\n" \
  "s_addc_u32 s91, s91, 0\nv_xor_b32 v192, 0, %[vc]\nv_min_u32 v192, %[lim], v192\ns_mov_b32 m0, s93\nv_add_u32 v192, v192, %[vrow]\ns_add_u32 s93, s93, 1024\n" \
  "global_load_lds_dwordx4 v192, s[90:91]\ns_add_u32 s90, s90, s92\ns_addc_u32 s91, s91, 0\nv_xor_b32 v192, 64, %[vc]\nv_min_u32 v192, %[lim], v192\ns_mov_b32 m0, s93\n" \
  "v_add_u32 v192, v192, %[vrow]\ns_add_u32 s93, s93, 1024\nglobal_load_lds_dwordx4 v192, s[90:91]\ns_add_u32 s90, s90, s92\ns_addc_u32 s91, s91, 0\nv_xor_b32 v192, 128, %[vc]\n" \
  "v_min_u32 v192, %[lim], v192\ns_mov_b32 m0, s93\nv_add_u32 v192, v192, %[vrow]\ns_add_u32 s93, s93, 1024\nglobal_load_lds_dwordx4 v192, s[90:91]\ns_add_u32 s90, s90, s92\n" \
  "s_addc_u32 s91, s91, 0\nv_xor_b32 v192, 192, %[vc]\nv_min_u32 v192, %[lim], v192\ns_mov_b32 m0, s93\nv_add_u32 v192, v192, %[vrow]\ns_add_u32 s93, s93, 1024\n" \
  "global_load_lds_dwordx4 v192, s[90:91]\ns_add_u32 s90, s90, s92\ns_addc_u32 s91, s91, 0\nv_xor_b32 v192, 128, %[vc]\nv_min_u32 v192, %[lim], v192\ns_mov_b32 m0, s93\n" \
  "v_add_u32 v192, v192, %[vrow]\ns_add_u32 s93, s93, 1024\nglobal_load_lds_dwordx4 v192, s[90:91]\ns_add_u32 s90, s90, s92\ns_addc_u32 s91, s91, 0\nv_xor_b32 v192, 192, %[vc]\n" \
  "v_min_u32 v192, %[lim], v192\ns_mov_b32 m0, s93\nv_add_u32 v192, v192, %[vrow]\ns_add_u32 s93, s93, 1024\nglobal_load_lds_dwordx4 v192, s[90:91]\ns_waitcnt vmcnt(16) lgkmcnt(0)\n" \
  "s_barrier\ns_cmp_eq_u32 %[px], -1\ns_cbranch_scc0 64f\nv_readfirstlane_b32 s90, v198\nv_readfirstlane_b32 s91, v199\ns_nop 3\ns_add_u32 s90, s90, 32\ns_addc_u32 %[shi], s91, 0\n" \
  "s_andn2_b32 s90, s90, 31\ns_or_b32 %[slo], %[slo], s90\ns_cmp_eq_u32 %[wave], 3\ns_cbranch_scc0 64f\nv_mov_b32 v198, %[slo]\nv_mov_b32 v199, %[shi]\nv_mov_b64 v[200:201], 0\n" \
  "s_mov_b64 exec, 0xff\nglobal_store_dwordx4 %[l16], v[198:201], %[me] sc1\ns_mov_b64 exec, -1\ns_nop 1\n64:\ns_mul_i32 s92, %[rs], 192\ns_mov_b64 s[90:91], %[w]\n" \
  "s_add_u32 s90, s90, s92\ns_addc_u32 s91, s91, 0\ns_lshl_b32 s93, %[wave], 13\ns_add_u32 s93, s93, 98304\ns_lshl_b32 s92, %[rs], 1\nv_xor_b32 v192, 0, %[vc]\n" \
  "v_min_u32 v192, %[lim], v192\ns_mov_b32 m0, s93\nv_add_u32 v192, v192, %[vrow]\ns_add_u32 s93, s93, 1024\nglobal_load_lds_dwordx4 v192, s[90:91]\ns_add_u32 s90, s90, s92\n" \
  "s_addc_u32 s91, s91, 0\nv_xor_b32 v192, 64, %[vc]\nv_min_u32 v192, %[lim], v192\ns_mov_b32 m0, s93\nv_add_u32 v192, v192, %[vrow]\ns_add_u32 s93, s93, 1024\n" \
  "global_load_lds_dwordx4 v192, s[90:91]\ns_add_u32 s90, s90, s92\ns_addc_u32 s91, s91, 0\nv_xor_b32 v192, 0, %[vc]\nv_min_u32 v192, %[lim], v192\ns_mov_b32 m0, s93\n" \
  "v_add_u32 v192, v192, %[vrow]\ns_add_u32 s93, s93, 1024\nglobal_load_lds_dwordx4 v192, s[90:91]\ns_add_u32 s90, s90, s92\ns_addc_u32 s91, s91, 0\nv_xor_b32 v192, 64, %[vc]\n" \
  "v_min_u32 v192, %[lim], v192\ns_mov_b32 m0, s93\nv_add_u32 v192, v192, %[vrow]\ns_add_u32 s93, s93, 1024\nglobal_load_lds_dwordx4 v192, s[90:91]\ns_add_u32 s90, s90, s92\n" \
  "s_addc_u32 s91, s91, 0\nv_xor_b32 v192, 128, %[vc]\nv_min_u32 v192, %[lim], v192\ns_mov_b32 m0, s93\nv_add_u32 v192, v192, %[vrow]\ns_add_u32 s93, s93, 1024\n" \
  "global_load_lds_dwordx4 v192, s[90:91]\ns_add_u32 s90, s90, s92\ns_addc_u32 s91, s91, 0\nv_xor_b32 v192, 192, %[vc]\nv_min_u32 v192, %[lim], v192\ns_mov_b32 m0, s93\n" \
  "v_add_u32 v192, v192, %[vrow]\ns_add_u32 s93, s93, 1024\nglobal_load_lds_dwordx4 v192, s[90:91]\ns_add_u32 s90, s90, s92\ns_addc_u32 s91, s91, 0\nv_xor_b32 v192, 128, %[vc]\n" \
  "v_min_u32 v192, %[lim], v192\ns_mov_b32 m0, s93\nv_add_u32 v192, v192, %[vrow]\ns_add_u32 s93, s93, 1024\nglobal_load_lds_dwordx4 v192, s[90:91]\ns_add_u32 s90, s90, s92\n" \
  "s_addc_u32 s91, s91, 0\nv_xor_b32 v192, 192, %[vc]\nv_min_u32 v192, %[lim], v192\ns_mov_b32 m0, s93\nv_add_u32 v192, v192, %[vrow]\ns_add_u32 s93, s93, 1024\n" \
  "global_load_lds_dwordx4 v192, s[90:91]\nv_xor_b32 v193, 0, %[t0]\nv_add_u32 v193, 0, v193\nv_xor_b32 v194, 32, %[t0]\nv_add_u32 v194, 0, v194\nv_xor_b32 v195, 64, %[t0]\n" \
  "v_add_u32 v195, 0, v195\nv_xor_b32 v196, 96, %[t0]\nv_add_u32 v196, 0, v196\nds_read_b64_tr_b16 a[0:1], v193 offset:0\nds_read_b64_tr_b16 a[2:3], v193 offset:2048\n" \
  "ds_read_b64_tr_b16 a[4:5], v194 offset:0\nds_read_b64_tr_b16 a[6:7], v194 offset:2048\nds_read_b64_tr_b16 a[8:9], v195 offset:0\nds_read_b64_tr_b16 a[10:11], v195 offset:2048\n" \
  "ds_read_b64_tr_b16 a[12:13], v196 offset:0\nds_read_b64_tr_b16 a[14:15], v196 offset:2048\nds_read_b64_tr_b16 a[16:17], v193 offset:16384\nds_read_b64_tr_b16 a[18:19], v193 offset:18432\n" \
  "ds_read_b64_tr_b16 a[20:21], v194 offset:16384\nds_read_b64_tr_b16 a[22:23], v194 offset:18432\ns_waitcnt lgkmcnt(4)\nds_read_b64_tr_b16 a[24:25], v195 offset:16384\n" \
  "ds_read_b64_tr_b16 a[26:27], v195 offset:18432\nds_read_b64_tr_b16 a[28:29], v196 offset:16384\nds_read_b64_tr_b16 a[30:31], v196 offset:18432\ns_waitcnt vmcnt(16) lgkmcnt(0)\n" \
  "s_barrier\ns_mul_i32 s92, %[rs], 256\ns_mov_b64 s[90:91], %[w]\ns_add_u32 s90, s90, s92\ns_addc_u32 s91, s91, 0\ns_lshl_b32 s93, %[wave], 13\ns_add_u32 s93, s93, 0\n" \
  "s_lshl_b32 s92, %[rs], 1\nv_xor_b32 v192, 0, %[vc]\nv_min_u32 v192, %[lim], v192\ns_mov_b32 m0, s93\nv_add_u32 v192, v192, %[vrow]\ns_add_u32 s93, s93, 1024\n" \
  "global_load_lds_dwordx4 v192, s[90:91]\ns_add_u32 s90, s90, s92\ns_addc_u32 s91, s91, 0\nv_xor_b32 v192, 64, %[vc]\nv_min_u32 v192, %[lim], v192\ns_mov_b32 m0, s93\n" \
  "v_add_u32 v192, v192, %[vrow]\ns_add_u32 s93, s93, 1024\nglobal_load_lds_dwordx4 v192, s[90:91]\ns_add_u32 s90, s90, s92\ns_addc_u32 s91, s91, 0\nv_xor_b32 v192, 0, %[vc]\n" \
  "v_min_u32 v192, %[lim], v192\ns_mov_b32 m0, s93\nv_add_u32 v192, v192, %[vrow]\ns_add_u32 s93, s93, 1024\nglobal_load_lds_dwordx4 v192, s[90:91]\ns_add_u32 s90, s90, s92\n" \
  "s_addc_u32 s91, s91, 0\nv_xor_b32 v192, 64, %[vc]\nv_min_u32 v192, %[lim], v192\ns_mov_b32 m0, s93\nv_add_u32 v192, v192, %[vrow]\ns_add_u32 s93, s93, 1024\n" \
  "global_load_lds_dwordx4 v192, s[90:91]\ns_add_u32 s90, s90, s92\ns_addc_u32 s91, s91, 0\nv_xor_b32 v192, 128, %[vc]\nv_min_u32 v192, %[lim], v192\ns_mov_b32 m0, s93\n" \
  "v_add_u32 v192, v192, %[vrow]\ns_add_u32 s93, s93, 1024\nglobal_load_lds_dwordx4 v192, s[90:91]\ns_add_u32 s90, s90, s92\ns_addc_u32 s91, s91, 0\nv_xor_b32 v192, 192, %[vc]\n" \
  "v_min_u32 v192, %[lim], v192\ns_mov_b32 m0, s93\nv_add_u32 v192, v192, %[vrow]\ns_add_u32 s93, s93, 1024\nglobal_load_lds_dwordx4 v192, s[90:91]\ns_add_u32 s90, s90, s92\n" \
  "s_addc_u32 s91, s91, 0\nv_xor_b32 v192, 128, %[vc]\nv_min_u32 v192, %[lim], v192\ns_mov_b32 m0, s93\nv_add_u32 v192, v192, %[vrow]\ns_add_u32 s93, s93, 1024\n" \
  "global_load_lds_dwordx4 v192, s[90:91]\ns_add_u32 s90, s90, s92\ns_addc_u32 s91, s91, 0\nv_xor_b32 v192, 192, %[vc]\nv_min_u32 v192, %[lim], v192\ns_mov_b32 m0, s93\n" \
  "v_add_u32 v192, v192, %[vrow]\ns_add_u32 s93, s93, 1024\nglobal_load_lds_dwordx4 v192, s[90:91]\nv_xor_b32 v193, 0, %[t0]\nv_add_u32 v193, 32768, v193\n" \
  "v_xor_b32 v194, 32, %[t0]\nv_add_u32 v194, 32768, v194\nv_xor_b32 v195, 64, %[t0]\nv_add_u32 v195, 32768, v195\nv_xor_b32 v196, 96, %[t0]\nv_add_u32 v196, 32768, v196\n" \
  "ds_read_b64_tr_b16 a[32:33], v193 offset:0\nds_read_b64_tr_b16 a[34:35], v193 offset:2048\nds_read_b64_tr_b16 a[36:37], v194 offset:0\nds_read_b64_tr_b16 a[38:39], v194 offset:2048\n" \
  "ds_read_b64_tr_b16 a[40:41], v195 offset:0\nds_read_b64_tr_b16 a[42:43], v195 offset:2048\nds_read_b64_tr_b16 a[44:45], v196 offset:0\nds_read_b64_tr_b16 a[46:47], v196 offset:2048\n" \
  "ds_read_b64_tr_b16 a[48:49], v193 offset:16384\nds_read_b64_tr_b16 a[50:51], v193 offset:18432\nds_read_b64_tr_b16 a[52:53], v194 offset:16384\nds_read_b64_tr_b16 a[54:55], v194 offset:18432\n" \
  "s_waitcnt lgkmcnt(4)\nds_read_b64_tr_b16 a[56:57], v195 offset:16384\nds_read_b64_tr_b16 a[58:59], v195 offset:18432\nds_read_b64_tr_b16 a[60:61], v196 offset:16384\n" \
  "ds_read_b64_tr_b16 a[62:63], v196 offset:18432\ns_waitcnt vmcnt(16) lgkmcnt(0)\ns_barrier\ns_mul_i32 s92, %[rs], 320\ns_mov_b64 s[90:91], %[w]\ns_add_u32 s90, s90, s92\n" \
  "s_addc_u32 s91, s91, 0\ns_lshl_b32 s93, %[wave], 13\ns_add_u32 s93, s93, 32768\ns_lshl_b32 s92, %[rs], 1\nv_xor_b32 v192, 0, %[vc]\nv_min_u32 v192, %[lim], v192\n" \
  "s_mov_b32 m0, s93\nv_add_u32 v192, v192, %[vrow]\ns_add_u32 s93, s93, 1024\nglobal_load_lds_dwordx4 v192, s[90:91]\ns_add_u32 s90, s90, s92\ns_addc_u32 s91, s91, 0\n" \
  "v_xor_b32 v192, 64, %[vc]\nv_min_u32 v192, %[lim], v192\ns_mov_b32 m0, s93\nv_add_u32 v192, v192, %[vrow]\ns_add_u32 s93, s93, 1024\nglobal_load_lds_dwordx4 v192, s[90:91]\n" \
  "s_add_u32 s90, s90, s92\ns_addc_u32 s91, s91, 0\nv_xor_b32 v192, 0, %[vc]\nv_min_u32 v192, %[lim], v192\ns_mov_b32 m0, s93\nv_add_u32 v192, v192, %[vrow]\n" \
  "s_add_u32 s93, s93, 1024\nglobal_load_lds_dwordx4 v192, s[90:91]\ns_add_u32 s90, s90, s92\ns_addc_u32 s91, s91, 0\nv_xor_b32 v192, 64, %[vc]\nv_min_u32 v192, %[lim], v192\n" \
  "s_mov_b32 m0, s93\nv_add_u32 v192, v192, %[vrow]\ns_add_u32 s93, s93, 1024\nglobal_load_lds_dwordx4 v192, s[90:91]\ns_add_u32 s90, s90, s92\ns_addc_u32 s91, s91, 0\n" \
  "v_xor_b32 v192, 128, %[vc]\nv_min_u32 v192, %[lim], v192\ns_mov_b32 m0, s93\nv_add_u32 v192, v192, %[vrow]\ns_add_u32 s93, s93, 1024\nglobal_load_lds_dwordx4 v192, s[90:91]\n" \
  "s_add_u32 s90, s90, s92\ns_addc_u32 s91, s91, 0\nv_xor_b32 v192, 192, %[vc]\nv_min_u32 v192, %[lim], v192\ns_mov_b32 m0, s93\nv_add_u32 v192, v192, %[vrow]\n" \
  "s_add_u32 s93, s93, 1024\nglobal_load_lds_dwordx4 v192, s[90:91]\ns_add_u32 s90, s90, s92\ns_addc_u32 s91, s91, 0\nv_xor_b32 v192, 128, %[vc]\nv_min_u32 v192, %[lim], v192\n" \
  "s_mov_b32 m0, s93\nv_add_u32 v192, v192, %[vrow]\ns_add_u32 s93, s93, 1024\nglobal_load_lds_dwordx4 v192, s[90:91]\ns_add_u32 s90, s90, s92\ns_addc_u32 s91, s91, 0\n" \
  "v_xor_b32 v192, 192, %[vc]\nv_min_u32 v192, %[lim], v192\ns_mov_b32 m0, s93\nv_add_u32 v192, v192, %[vrow]\ns_add_u32 s93, s93, 1024\nglobal_load_lds_dwordx4 v192, s[90:91]\n" \
  "v_xor_b32 v193, 0, %[t0]\nv_add_u32 v193, 65536, v193\nv_xor_b32 v194, 32, %[t0]\nv_add_u32 v194, 65536, v194\nv_xor_b32 v195, 64, %[t0]\nv_add_u32 v195, 65536, v195\n" \
  "v_xor_b32 v196, 96, %[t0]\nv_add_u32 v196, 65536, v196\nds_read_b64_tr_b16 a[64:65], v193 offset:0\nds_read_b64_tr_b16 a[66:67], v193 offset:2048\nds_read_b64_tr_b16 a[68:69], v194 offset:0\n" \
  "ds_read_b64_tr_b16 a[70:71], v194 offset:2048\nds_read_b64_tr_b16 a[72:73], v195 offset:0\nds_read_b64_tr_b16 a[74:75], v195 offset:2048\nds_read_b64_tr_b16 a[76:77], v196 offset:0\n" \
  "ds_read_b64_tr_b16 a[78:79], v196 offset:2048\nds_read_b64_tr_b16 a[80:81], v193 offset:16384\nds_read_b64_tr_b16 a[82:83], v193 offset:18432\nds_read_b64_tr_b16 a[84:85], v194 offset:16384\n" \
  "ds_read_b64_tr_b16 a[86:87], v194 offset:18432\ns_waitcnt lgkmcnt(4)\nds_read_b64_tr_b16 a[88:89], v195 offset:16384\nds_read_b64_tr_b16 a[90:91], v195 offset:18432\n" \
  "ds_read_b64_tr_b16 a[92:93], v196 offset:16384\nds_read_b64_tr_b16 a[94:95], v196 offset:18432\ns_waitcnt vmcnt(16) lgkmcnt(0)\ns_barrier\ns_mul_i32 s92, %[rs], 384\n" \
  "s_mov_b64 s[90:91], %[w]\ns_add_u32 s90, s90, s92\ns_addc_u32 s91, s91, 0\ns_lshl_b32 s93, %[wave], 13\ns_add_u32 s93, s93, 65536\ns_lshl_b32 s92, %[rs], 1\n" \
  "v_xor_b32 v192, 0, %[vc]\nv_min_u32 v192, %[lim], v192\ns_mov_b32 m0, s93\nv_add_u32 v192, v192, %[vrow]\ns_add_u32 s93, s93, 1024\nglobal_load_lds_dwordx4 v192, s[90:91]\n" \
  "s_add_u32 s90, s90, s92\ns_addc_u32 s91, s91, 0\nv_xor_b32 v192, 64, %[vc]\nv_min_u32 v192, %[lim], v192\ns_mov_b32 m0, s93\nv_add_u32 v192, v192, %[vrow]\n" \
  "s_add_u32 s93, s93, 1024\nglobal_load_lds_dwordx4 v192, s[90:91]\ns_add_u32 s90, s90, s92\ns_addc_u32 s91, s91, 0\nv_xor_b32 v192, 0, %[vc]\nv_min_u32 v192, %[lim], v192\n" \
  "s_mov_b32 m0, s93\nv_add_u32 v192, v192, %[vrow]\ns_add_u32 s93, s93, 1024\nglobal_load_lds_dwordx4 v192, s[90:91]\ns_add_u32 s90, s90, s92\ns_addc_u32 s91, s91, 0\n" \
  "v_xor_b32 v192, 64, %[vc]\nv_min_u32 v192, %[lim], v192\ns_mov_b32 m0, s93\nv_add_u32 v192, v192, %[vrow]\ns_add_u32 s93, s93, 1024\nglobal_load_lds_dwordx4 v192, s[90:91]\n" \
  "s_add_u32 s90, s90, s92\ns_addc_u32 s91, s91, 0\nv_xor_b32 v192, 128, %[vc]\nv_min_u32 v192, %[lim], v192\ns_mov_b32 m0, s93\nv_add_u32 v192, v192, %[vrow]\n" \
  "s_add_u32 s93, s93, 1024\nglobal_load_lds_dwordx4 v192, s[90:91]\ns_add_u32 s90, s90, s92\ns_addc_u32 s91, s91, 0\nv_xor_b32 v192, 192, %[vc]\nv_min_u32 v192, %[lim], v192\n" \
  "s_mov_b32 m0, s93\nv_add_u32 v192, v192, %[vrow]\ns_add_u32 s93, s93, 1024\nglobal_load_lds_dwordx4 v192, s[90:91]\ns_add_u32 s90, s90, s92\ns_addc_u32 s91, s91, 0\n" \
  "v_xor_b32 v192, 128, %[vc]\nv_min_u32 v192, %[lim], v192\ns_mov_b32 m0, s93\nv_add_u32 v192, v192, %[vrow]\ns_add_u32 s93, s93, 1024\nglobal_load_lds_dwordx4 v192, s[90:91]\n" \
  "s_add_u32 s90, s90, s92\ns_addc_u32 s91, s91, 0\nv_xor_b32 v192, 192, %[vc]\nv_min_u32 v192, %[lim], v192\ns_mov_b32 m0, s93\nv_add_u32 v192, v192, %[vrow]\n" \
  "s_add_u32 s93, s93, 1024\nglobal_load_lds_dwordx4 v192, s[90:91]\nv_xor_b32 v193, 0, %[t0]\nv_add_u32 v193, 98304, v193\nv_xor_b32 v194, 32, %[t0]\nv_add_u32 v194, 98304, v194\n" \
  "v_xor_b32 v195, 64, %[t0]\nv_add_u32 v195, 98304, v195\nv_xor_b32 v196, 96, %[t0]\nv_add_u32 v196, 98304, v196\nds_read_b64_tr_b16 a[96:97], v193 offset:0\n" \
  "ds_read_b64_tr_b16 a[98:99], v193 offset:2048\nds_read_b64_tr_b16 a[100:101], v194 offset:0\nds_read_b64_tr_b16 a[102:103], v194 offset:2048\nds_read_b64_tr_b16 a[104:105], v195 offset:0\n" \
  "ds_read_b64_tr_b16 a[106:107], v195 offset:2048\nds_read_b64_tr_b16 a[108:109], v196 offset:0\nds_read_b64_tr_b16 a[110:111], v196 offset:2048\nds_read_b64_tr_b16 a[112:113], v193 offset:16384\n" \
  "ds_read_b64_tr_b16 a[114:115], v193 offset:18432\nds_read_b64_tr_b16 a[116:117], v194 offset:16384\nds_read_b64_tr_b16 a[118:119], v194 offset:18432\ns_waitcnt lgkmcnt(4)\n" \
  "ds_read_b64_tr_b16 a[120:121], v195 offset:16384\nds_read_b64_tr_b16 a[122:123], v195 offset:18432\nds_read_b64_tr_b16 a[124:125], v196 offset:16384\nds_read_b64_tr_b16 a[126:127], v196 offset:18432\n" \
  "s_waitcnt vmcnt(16) lgkmcnt(0)\ns_barrier\ns_mul_i32 s92, %[rs], 448\ns_mov_b64 s[90:91], %[w]\ns_add_u32 s90, s90, s92\ns_addc_u32 s91, s91, 0\ns_lshl_b32 s93, %[wave], 13\n" \
  "s_add_u32 s93, s93, 98304\ns_lshl_b32 s92, %[rs], 1\nv_xor_b32 v192, 0, %[vc]\nv_min_u32 v192, %[lim], v192\ns_mov_b32 m0, s93\nv_add_u32 v192, v192, %[vrow]\n" \
  "s_add_u32 s93, s93, 1024\nglobal_load_lds_dwordx4 v192, s[90:91]\ns_add_u32 s90, s90, s92\ns_addc_u32 s91, s91, 0\nv_xor_b32 v192, 64, %[vc]\nv_min_u32 v192, %[lim], v192\n" \
  "s_mov_b32 m0, s93\nv_add_u32 v192, v192, %[vrow]\ns_add_u32 s93, s93, 1024\nglobal_load_lds_dwordx4 v192, s[90:91]\ns_add_u32 s90, s90, s92\ns_addc_u32 s91, s91, 0\n" \
  "v_xor_b32 v192, 0, %[vc]\nv_min_u32 v192, %[lim], v192\ns_mov_b32 m0, s93\nv_add_u32 v192, v192, %[vrow]\ns_add_u32 s93, s93, 1024\nglobal_load_lds_dwordx4 v192, s[90:91]\n" \
  "s_add_u32 s90, s90, s92\ns_addc_u32 s91, s91, 0\nv_xor_b32 v192, 64, %[vc]\nv_min_u32 v192, %[lim], v192\ns_mov_b32 m0, s93\nv_add_u32 v192, v192, %[vrow]\n" \
  "s_add_u32 s93, s93, 1024\nglobal_load_lds_dwordx4 v192, s[90:91]\ns_add_u32 s90, s90, s92\ns_addc_u32 s91, s91, 0\nv_xor_b32 v192, 128, %[vc]\nv_min_u32 v192, %[lim], v192\n" \
  "s_mov_b32 m0, s93\nv_add_u32 v192, v192, %[vrow]\ns_add_u32 s93, s93, 1024\nglobal_load_lds_dwordx4 v192, s[90:91]\ns_add_u32 s90, s90, s92\ns_addc_u32 s91, s91, 0\n" \
  "v_xor_b32 v192, 192, %[vc]\nv_min_u32 v192, %[lim], v192\ns_mov_b32 m0, s93\nv_add_u32 v192, v192, %[vrow]\ns_add_u32 s93, s93, 1024\nglobal_load_lds_dwordx4 v192, s[90:91]\n" \
  "s_add_u32 s90, s90, s92\ns_addc_u32 s91, s91, 0\nv_xor_b32 v192, 128, %[vc]\nv_min_u32 v192, %[lim], v192\ns_mov_b32 m0, s93\nv_add_u32 v192, v192, %[vrow]\n" \
  "s_add_u32 s93, s93, 1024\nglobal_load_lds_dwordx4 v192, s[90:91]\ns_add_u32 s90, s90, s92\ns_addc_u32 s91, s91, 0\nv_xor_b32 v192, 192, %[vc]\nv_min_u32 v192, %[lim], v192\n" \
  "s_mov_b32 m0, s93\nv_add_u32 v192, v192, %[vrow]\ns_add_u32 s93, s93, 1024\nglobal_load_lds_dwordx4 v192, s[90:91]\nv_xor_b32 v193, 0, %[t0]\nv_add_u32 v193, 0, v193\n" \
  "v_xor_b32 v194, 32, %[t0]\nv_add_u32 v194, 0, v194\nv_xor_b32 v195, 64, %[t0]\nv_add_u32 v195, 0, v195\nv_xor_b32 v196, 96, %[t0]\nv_add_u32 v196, 0, v196\n" \
  "ds_read_b64_tr_b16 a[128:129], v193 offset:0\nds_read_b64_tr_b16 a[130:131], v193 offset:2048\nds_read_b64_tr_b16 a[132:133], v194 offset:0\nds_read_b64_tr_b16 a[134:135], v194 offset:2048\n" \
  "ds_read_b64_tr_b16 a[136:137], v195 offset:0\nds_read_b64_tr_b16 a[138:139], v195 offset:2048\nds_read_b64_tr_b16 a[140:141], v196 offset:0\nds_read_b64_tr_b16 a[142:143], v196 offset:2048\n" \
  "ds_read_b64_tr_b16 a[144:145], v193 offset:16384\nds_read_b64_tr_b16 a[146:147], v193 offset:18432\nds_read_b64_tr_b16 a[148:149], v194 offset:16384\nds_read_b64_tr_b16 a[150:151], v194 offset:18432\n" \
  "s_waitcnt lgkmcnt(4)\nds_read_b64_tr_b16 a[152:153], v195 offset:16384\nds_read_b64_tr_b16 a[154:155], v195 offset:18432\nds_read_b64_tr_b16 a[156:157], v196 offset:16384\n" \
  "ds_read_b64_tr_b16 a[158:159], v196 offset:18432\ns_waitcnt vmcnt(16) lgkmcnt(0)\ns_barrier\ns_mul_i32 s92, %[rs], 512\ns_mov_b64 s[90:91], %[w]\ns_add_u32 s90, s90, s92\n" \
  "s_addc_u32 s91, s91, 0\ns_lshl_b32 s93, %[wave], 13\ns_add_u32 s93, s93, 0\ns_lshl_b32 s92, %[rs], 1\nv_xor_b32 v192, 0, %[vc]\nv_min_u32 v192, %[lim], v192\n" \
  "s_mov_b32 m0, s93\nv_add_u32 v192, v192, %[vrow]\ns_add_u32 s93, s93, 1024\nglobal_load_lds_dwordx4 v192, s[90:91]\ns_add_u32 s90, s90, s92\ns_addc_u32 s91, s91, 0\n" \
  "v_xor_b32 v192, 64, %[vc]\nv_min_u32 v192, %[lim], v192\ns_mov_b32 m0, s93\nv_add_u32 v192, v192, %[vrow]\ns_add_u32 s93, s93, 1024\nglobal_load_lds_dwordx4 v192, s[90:91]\n" \
  "s_add_u32 s90, s90, s92\ns_addc_u32 s91, s91, 0\nv_xor_b32 v192, 0, %[vc]\nv_min_u32 v192, %[lim], v192\ns_mov_b32 m0, s93\nv_add_u32 v192, v192, %[vrow]\n" \
  "s_add_u32 s93, s93, 1024\nglobal_load_lds_dwordx4 v192, s[90:91]\ns_add_u32 s90, s90, s92\ns_addc_u32 s91, s91, 0\nv_xor_b32 v192, 64, %[vc]\nv_min_u32 v192, %[lim], v192\n" \
  "s_mov_b32 m0, s93\nv_add_u32 v192, v192, %[vrow]\ns_add_u32 s93, s93, 1024\nglobal_load_lds_dwordx4 v192, s[90:91]\ns_add_u32 s90, s90, s92\ns_addc_u32 s91, s91, 0\n" \
  "v_xor_b32 v192, 128, %[vc]\nv_min_u32 v192, %[lim], v192\ns_mov_b32 m0, s93\nv_add_u32 v192, v192, %[vrow]\ns_add_u32 s93, s93, 1024\nglobal_load_lds_dwordx4 v192, s[90:91]\n" \
  "s_add_u32 s90, s90, s92\ns_addc_u32 s91, s91, 0\nv_xor_b32 v192, 192, %[vc]\nv_min_u32 v192, %[lim], v192\ns_mov_b32 m0, s93\nv_add_u32 v192, v192, %[vrow]\n" \
  "s_add_u32 s93, s93, 1024\nglobal_load_lds_dwordx4 v192, s[90:91]\ns_add_u32 s90, s90, s92\ns_addc_u32 s91, s91, 0\nv_xor_b32 v192, 128, %[vc]\nv_min_u32 v192, %[lim], v192\n" \
  "s_mov_b32 m0, s93\nv_add_u32 v192, v192, %[vrow]\ns_add_u32 s93, s93, 1024\nglobal_load_lds_dwordx4 v192, s[90:91]\ns_add_u32 s90, s90, s92\ns_addc_u32 s91, s91, 0\n" \
  "v_xor_b32 v192, 192, %[vc]\nv_min_u32 v192, %[lim], v192\ns_mov_b32 m0, s93\nv_add_u32 v192, v192, %[vrow]\ns_add_u32 s93, s93, 1024\nglobal_load_lds_dwordx4 v192, s[90:91]\n" \
  "v_xor_b32 v193, 0, %[t0]\nv_add_u32 v193, 32768, v193\nv_xor_b32 v194, 32, %[t0]\nv_add_u32 v194, 32768, v194\nv_xor_b32 v195, 64, %[t0]\nv_add_u32 v195, 32768, v195\n" \
  "v_xor_b32 v196, 96, %[t0]\nv_add_u32 v196, 32768, v196\nds_read_b64_tr_b16 a[160:161], v193 offset:0\nds_read_b64_tr_b16 a[162:163], v193 offset:2048\n" \
  "ds_read_b64_tr_b16 a[164:165], v194 offset:0\nds_read_b64_tr_b16 a[166:167], v194 offset:2048\nds_read_b64_tr_b16 a[168:169], v195 offset:0\nds_read_b64_tr_b16 a[170:171], v195 offset:2048\n" \
  "ds_read_b64_tr_b16 a[172:173], v196 offset:0\nds_read_b64_tr_b16 a[174:175], v196 offset:2048\nds_read_b64_tr_b16 a[176:177], v193 offset:16384\nds_read_b64_tr_b16 a[178:179], v193 offset:18432\n" \
  "ds_read_b64_tr_b16 a[180:181], v194 offset:16384\nds_read_b64_tr_b16 a[182:183], v194 offset:18432\ns_waitcnt lgkmcnt(4)\nds_read_b64_tr_b16 a[184:185], v195 offset:16384\n" \
  "ds_read_b64_tr_b16 a[186:187], v195 offset:18432\nds_read_b64_tr_b16 a[188:189], v196 offset:16384\nds_read_b64_tr_b16 a[190:191], v196 offset:18432\ns_waitcnt vmcnt(16) lgkmcnt(0)\n" \
  "s_barrier\ns_mul_i32 s92, %[rs], 576\ns_mov_b64 s[90:91], %[w]\ns_add_u32 s90, s90, s92\ns_addc_u32 s91, s91, 0\ns_lshl_b32 s93, %[wave], 13\ns_add_u32 s93, s93, 32768\n" \
  "s_lshl_b32 s92, %[rs], 1\nv_xor_b32 v192, 0, %[vc]\nv_min_u32 v192, %[lim], v192\ns_mov_b32 m0, s93\nv_add_u32 v192, v192, %[vrow]\ns_add_u32 s93, s93, 1024\n" \
  "global_load_lds_dwordx4 v192, s[90:91]\ns_add_u32 s90, s90, s92\ns_addc_u32 s91, s91, 0\nv_xor_b32 v192, 64, %[vc]\nv_min_u32 v192, %[lim], v192\ns_mov_b32 m0, s93\n" \
  "v_add_u32 v192, v192, %[vrow]\ns_add_u32 s93, s93, 1024\nglobal_load_lds_dwordx4 v192, s[90:91]\ns_add_u32 s90, s90, s92\ns_addc_u32 s91, s91, 0\nv_xor_b32 v192, 0, %[vc]\n" \
  "v_min_u32 v192, %[lim], v192\ns_mov_b32 m0, s93\nv_add_u32 v192, v192, %[vrow]\ns_add_u32 s93, s93, 1024\nglobal_load_lds_dwordx4 v192, s[90:91]\ns_add_u32 s90, s90, s92\n" \
  "s_addc_u32 s91, s91, 0\nv_xor_b32 v192, 64, %[vc]\nv_min_u32 v192, %[lim], v192\ns_mov_b32 m0, s93\nv_add_u32 v192, v192, %[vrow]\ns_add_u32 s93, s93, 1024\n" \
  "global_load_lds_dwordx4 v192, s[90:91]\ns_add_u32 s90, s90, s92\ns_addc_u32 s91, s91, 0\nv_xor_b32 v192, 128, %[vc]\nv_min_u32 v192, %[lim], v192\ns_mov_b32 m0, s93\n" \
  "v_add_u32 v192, v192, %[vrow]\ns_add_u32 s93, s93, 1024\nglobal_load_lds_dwordx4 v192, s[90:91]\ns_add_u32 s90, s90, s92\ns_addc_u32 s91, s91, 0\nv_xor_b32 v192, 192, %[vc]\n" \
  "v_min_u32 v192, %[lim], v192\ns_mov_b32 m0, s93\nv_add_u32 v192, v192, %[vrow]\ns_add_u32 s93, s93, 1024\nglobal_load_lds_dwordx4 v192, s[90:91]\ns_add_u32 s90, s90, s92\n" \
  "s_addc_u32 s91, s91, 0\nv_xor_b32 v192, 128, %[vc]\nv_min_u32 v192, %[lim], v192\ns_mov_b32 m0, s93\nv_add_u32 v192, v192, %[vrow]\ns_add_u32 s93, s93, 1024\n" \
  "global_load_lds_dwordx4 v192, s[90:91]\ns_add_u32 s90, s90, s92\ns_addc_u32 s91, s91, 0\nv_xor_b32 v192, 192, %[vc]\nv_min_u32 v192, %[lim], v192\ns_mov_b32 m0, s93\n" \
  "v_add_u32 v192, v192, %[vrow]\ns_add_u32 s93, s93, 1024\nglobal_load_lds_dwordx4 v192, s[90:91]\nv_xor_b32 v193, 0, %[t0]\nv_add_u32 v193, 65536, v193\n" \
  "v_xor_b32 v194, 32, %[t0]\nv_add_u32 v194, 65536, v194\nv_xor_b32 v195, 64, %[t0]\nv_add_u32 v195, 65536, v195\nv_xor_b32 v196, 96, %[t0]\nv_add_u32 v196, 65536, v196\n" \
  "ds_read_b64_tr_b16 a[192:193], v193 offset:0\nds_read_b64_tr_b16 a[194:195], v193 offset:2048\nds_read_b64_tr_b16 a[196:197], v194 offset:0\nds_read_b64_tr_b16 a[198:199], v194 offset:2048\n" \
  "ds_read_b64_tr_b16 a[200:201], v195 offset:0\nds_read_b64_tr_b16 a[202:203], v195 offset:2048\nds_read_b64_tr_b16 a[204:205], v196 offset:0\nds_read_b64_tr_b16 a[206:207], v196 offset:2048\n" \
  "ds_read_b64_tr_b16 a[208:209], v193 offset:16384\nds_read_b64_tr_b16 a[210:211], v193 offset:18432\nds_read_b64_tr_b16 a[212:213], v194 offset:16384\nds_read_b64_tr_b16 a[214:215], v194 offset:18432\n" \
  "s_waitcnt lgkmcnt(4)\nds_read_b64_tr_b16 a[216:217], v195 offset:16384\nds_read_b64_tr_b16 a[218:219], v195 offset:18432\nds_read_b64_tr_b16 a[220:221], v196 offset:16384\n" \
  "ds_read_b64_tr_b16 a[222:223], v196 offset:18432\ns_waitcnt vmcnt(16) lgkmcnt(0)\ns_barrier\nv_xor_b32 v193, 0, %[t0]\nv_add_u32 v193, 98304, v193\nv_xor_b32 v194, 32, %[t0]\n" \
  "v_add_u32 v194, 98304, v194\nv_xor_b32 v195, 64, %[t0]\nv_add_u32 v195, 98304, v195\nv_xor_b32 v196, 96, %[t0]\nv_add_u32 v196, 98304, v196\nds_read_b64_tr_b16 a[224:225], v193 offset:0\n" \
  "ds_read_b64_tr_b16 a[226:227], v193 offset:2048\nds_read_b64_tr_b16 a[228:229], v194 offset:0\nds_read_b64_tr_b16 a[230:231], v194 offset:2048\nds_read_b64_tr_b16 a[232:233], v195 offset:0\n" \
  "ds_read_b64_tr_b16 a[234:235], v195 offset:2048\nds_read_b64_tr_b16 a[236:237], v196 offset:0\nds_read_b64_tr_b16 a[238:239], v196 offset:2048\nds_read_b64_tr_b16 a[240:241], v193 offset:16384\n" \
  "ds_read_b64_tr_b16 a[242:243], v193 offset:18432\nds_read_b64_tr_b16 a[244:245], v194 offset:16384\nds_read_b64_tr_b16 a[246:247], v194 offset:18432\ns_waitcnt lgkmcnt(4)\n" \
  "ds_read_b64_tr_b16 a[248:249], v195 offset:16384\nds_read_b64_tr_b16 a[250:251], v195 offset:18432\nds_read_b64_tr_b16 a[252:253], v196 offset:16384\nds_read_b64_tr_b16 a[254:255], v196 offset:18432\n" \
  "s_waitcnt vmcnt(8) lgkmcnt(0)\ns_barrier\nv_xor_b32 v193, 0, %[t0]\nv_add_u32 v193, 0, v193\nv_xor_b32 v194, 32, %[t0]\nv_add_u32 v194, 0, v194\nv_xor_b32 v195, 64, %[t0]\n" \
  "v_add_u32 v195, 0, v195\nv_xor_b32 v196, 96, %[t0]\nv_add_u32 v196, 0, v196\nds_read_b64_tr_b16 v[128:129], v193 offset:0\nds_read_b64_tr_b16 v[130:131], v193 offset:2048\n" \
  "ds_read_b64_tr_b16 v[132:133], v194 offset:0\nds_read_b64_tr_b16 v[134:135], v194 offset:2048\nds_read_b64_tr_b16 v[136:137], v195 offset:0\nds_read_b64_tr_b16 v[138:139], v195 offset:2048\n" \
  "ds_read_b64_tr_b16 v[140:141], v196 offset:0\nds_read_b64_tr_b16 v[142:143], v196 offset:2048\nds_read_b64_tr_b16 v[144:145], v193 offset:16384\nds_read_b64_tr_b16 v[146:147], v193 offset:18432\n" \
  "ds_read_b64_tr_b16 v[148:149], v194 offset:16384\nds_read_b64_tr_b16 v[150:151], v194 offset:18432\ns_waitcnt lgkmcnt(4)\nds_read_b64_tr_b16 v[152:153], v195 offset:16384\n" \
  "ds_read_b64_tr_b16 v[154:155], v195 offset:18432\nds_read_b64_tr_b16 v[156:157], v196 offset:16384\nds_read_b64_tr_b16 v[158:159], v196 offset:18432\ns_waitcnt vmcnt(0) lgkmcnt(0)\n" \
  "s_barrier\nv_xor_b32 v193, 0, %[t0]\nv_add_u32 v193, 32768, v193\nv_xor_b32 v194, 32, %[t0]\nv_add_u32 v194, 32768, v194\nv_xor_b32 v195, 64, %[t0]\nv_add_u32 v195, 32768, v195\n" \
  "v_xor_b32 v196, 96, %[t0]\nv_add_u32 v196, 32768, v196\nds_read_b64_tr_b16 v[160:161], v193 offset:0\nds_read_b64_tr_b16 v[162:163], v193 offset:2048\n" \
  "ds_read_b64_tr_b16 v[164:165], v194 offset:0\nds_read_b64_tr_b16 v[166:167], v194 offset:2048\nds_read_b64_tr_b16 v[168:169], v195 offset:0\nds_read_b64_tr_b16 v[170:171], v195 offset:2048\n" \
  "ds_read_b64_tr_b16 v[172:173], v196 offset:0\nds_read_b64_tr_b16 v[174:175], v196 offset:2048\nds_read_b64_tr_b16 v[176:177], v193 offset:16384\nds_read_b64_tr_b16 v[178:179], v193 offset:18432\n" \
  "ds_read_b64_tr_b16 v[180:181], v194 offset:16384\nds_read_b64_tr_b16 v[182:183], v194 offset:18432\ns_waitcnt lgkmcnt(4)\nds_read_b64_tr_b16 v[184:185], v195 offset:16384\n" \
  "ds_read_b64_tr_b16 v[186:187], v195 offset:18432\nds_read_b64_tr_b16 v[188:189], v196 offset:16384\nds_read_b64_tr_b16 v[190:191], v196 offset:18432\ns_waitcnt lgkmcnt(0)\n" \
  "s_mov_b32 s93, 0x200000\n60:\nglobal_load_dwordx2 v[192:193], %[aw], %[ab] sc1\ns_waitcnt vmcnt(0)\nv_xor_b32 v192, %[slo], v192\nv_xor_b32 v193, %[shi], v193\n" \
  "v_lshrrev_b32 v192, 5, v192\nv_or_b32 v192, v192, v193\nv_cmp_ne_u32 vcc, 0, v192\ns_cbranch_vccz 61f\ns_sleep 4\ns_sub_u32 s93, s93, 1\ns_cbranch_scc0 60b\n" \
  "61:\ns_barrier\ns_mov_b64 s[90:91], %[src]\ns_mov_b32 m0, %[dst]\ns_add_u32 s92, %[dst], 4096\nglobal_load_lds_dwordx4 %[l16], s[90:91] sc1\nglobal_load_lds_dwordx4 %[l16], s[90:91] offset:1024 sc1\n" \
  "global_load_lds_dwordx4 %[l16], s[90:91] offset:2048 sc1\nglobal_load_lds_dwordx4 %[l16], s[90:91] offset:3072 sc1\ns_add_u32 s90, s90, 4096\ns_addc_u32 s91, s91, 0\n" \
  "s_mov_b32 m0, s92\ns_nop 0\nglobal_load_lds_dwordx4 %[l16], s[90:91] offset:0 sc1\nglobal_load_lds_dwordx4 %[l16], s[90:91] offset:1024 sc1\nglobal_load_lds_dwordx4 %[l16], s[90:91] offset:2048 sc1\n" \
  "global_load_lds_dwordx4 %[l16], s[90:91] offset:3072 sc1\ns_mov_b32 m0, %[dst2]\ns_nop 0\nglobal_load_lds_dwordx4 %[l16], %[src2] offset:0 sc1\nglobal_load_lds_dwordx4 %[l16], %[src2] offset:1024 sc1\n" \
  "ds_write_b128 %[bl], v[128:131] offset:0\nds_write_b128 %[bl], v[132:135] offset:1024\nds_write_b128 %[bl], v[136:139] offset:2048\nds_write_b128 %[bl], v[140:143] offset:3072\n" \
  "ds_write_b128 %[bl], v[144:147] offset:4096\nds_write_b128 %[bl], v[148:151] offset:5120\nds_write_b128 %[bl], v[152:155] offset:6144\nds_write_b128 %[bl], v[156:159] offset:7168\n" \
  "ds_write_b128 %[bl], v[160:163] offset:8192\nds_write_b128 %[bl], v[164:167] offset:9216\nds_write_b128 %[bl], v[168:171] offset:10240\nds_write_b128 %[bl], v[172:175] offset:11264\n" \
  "s_waitcnt lgkmcnt(4)\nds_write_b128 %[bl], v[176:179] offset:12288\nds_write_b128 %[bl], v[180:183] offset:13312\nds_write_b128 %[bl], v[184:187] offset:14336\n" \
  "ds_write_b128 %[bl], v[188:191] offset:15360\ns_waitcnt vmcnt(0) lgkmcnt(0)\ns_barrier\n"
#define R1_LOAD_CLOB \
  "v128","v129","v130","v131","v132","v133","v134","v135","v136","v137","v138","v139","v140","v141","v142","v143","v144","v145","v146","v147","v148","v149", \
  "v150","v151","v152","v153","v154","v155","v156","v157","v158","v159","v160","v161","v162","v163","v164","v165","v166","v167","v168","v169","v170","v171", \
  "v172","v173","v174","v175","v176","v177","v178","v179","v180","v181","v182","v183","v184","v185","v186","v187","v188","v189","v190","v191","v192","v193", \
  "v194","v195","v196","v197","v198","v199","v200","v201","s90","s91","s92","s93"
#define R1_MAIN_W_ASM \
  "ds_read_b128 v[128:131], %[al] offset:0\nds_read_b128 v[132:135], %[al] offset:1024\nds_read_b128 v[136:139], %[al] offset:2048\nds_read_b128 v[140:143], %[al] offset:3072\n" \
  "s_waitcnt lgkmcnt(2)\nv_mfma_f32_16x16x32_bf16 %[c0], a[0:3], v[128:131], 0\nds_read_b128 v[144:147], %[al] offset:4096\nv_mfma_f32_16x16x32_bf16 %[c1], a[4:7], v[128:131], 0\n" \
  "ds_read_b128 v[148:151], %[al] offset:5120\nv_mfma_f32_16x16x32_bf16 %[c2], a[8:11], v[128:131], 0\ns_mov_b64 s[90:91], %[src]\ns_mov_b32 m0, %[dst]\ns_add_u32 s92, %[dst], 4096\n" \
  "global_load_lds_dwordx4 %[l16], s[90:91] sc1\nv_mfma_f32_16x16x32_bf16 %[c3], a[12:15], v[128:131], 0\nv_mfma_f32_16x16x32_bf16 %[c4], a[0:3], v[132:135], 0\n" \
  "v_mfma_f32_16x16x32_bf16 %[c5], a[4:7], v[132:135], 0\nv_mfma_f32_16x16x32_bf16 %[c6], a[8:11], v[132:135], 0\nv_mfma_f32_16x16x32_bf16 %[c7], a[12:15], v[132:135], 0\n" \
  "s_waitcnt lgkmcnt(2)\nv_mfma_f32_16x16x32_bf16 %[c0], a[16:19], v[136:139], %[c0]\nds_read_b128 v[128:131], %[al] offset:6144\nv_mfma_f32_16x16x32_bf16 %[c1], a[20:23], v[136:139], %[c1]\n" \
  "ds_read_b128 v[132:135], %[al] offset:7168\nv_mfma_f32_16x16x32_bf16 %[c2], a[24:27], v[136:139], %[c2]\nglobal_load_lds_dwordx4 %[l16], s[90:91] offset:1024 sc1\n" \
  "v_mfma_f32_16x16x32_bf16 %[c3], a[28:31], v[136:139], %[c3]\nv_mfma_f32_16x16x32_bf16 %[c4], a[16:19], v[140:143], %[c4]\nv_mfma_f32_16x16x32_bf16 %[c5], a[20:23], v[140:143], %[c5]\n" \
  "v_mfma_f32_16x16x32_bf16 %[c6], a[24:27], v[140:143], %[c6]\nv_mfma_f32_16x16x32_bf16 %[c7], a[28:31], v[140:143], %[c7]\ns_waitcnt lgkmcnt(2)\nv_mfma_f32_16x16x32_bf16 %[c0], a[32:35], v[144:147], %[c0]\n" \
  "ds_read_b128 v[136:139], %[al] offset:8192\nv_mfma_f32_16x16x32_bf16 %[c1], a[36:39], v[144:147], %[c1]\nds_read_b128 v[140:143], %[al] offset:9216\nv_mfma_f32_16x16x32_bf16 %[c2], a[40:43], v[144:147], %[c2]\n" \
  "global_load_lds_dwordx4 %[l16], s[90:91] offset:2048 sc1\nv_mfma_f32_16x16x32_bf16 %[c3], a[44:47], v[144:147], %[c3]\nv_mfma_f32_16x16x32_bf16 %[c4], a[32:35], v[148:151], %[c4]\n" \
  "v_mfma_f32_16x16x32_bf16 %[c5], a[36:39], v[148:151], %[c5]\nv_mfma_f32_16x16x32_bf16 %[c6], a[40:43], v[148:151], %[c6]\nv_mfma_f32_16x16x32_bf16 %[c7], a[44:47], v[148:151], %[c7]\n" \
  "s_waitcnt lgkmcnt(2)\nv_mfma_f32_16x16x32_bf16 %[c0], a[48:51], v[128:131], %[c0]\nds_read_b128 v[144:147], %[al] offset:10240\nv_mfma_f32_16x16x32_bf16 %[c1], a[52:55], v[128:131], %[c1]\n" \
  "ds_read_b128 v[148:151], %[al] offset:11264\nv_mfma_f32_16x16x32_bf16 %[c2], a[56:59], v[128:131], %[c2]\nglobal_load_lds_dwordx4 %[l16], s[90:91] offset:3072 sc1\n" \
  "v_mfma_f32_16x16x32_bf16 %[c3], a[60:63], v[128:131], %[c3]\nv_mfma_f32_16x16x32_bf16 %[c4], a[48:51], v[132:135], %[c4]\nv_mfma_f32_16x16x32_bf16 %[c5], a[52:55], v[132:135], %[c5]\n" \
  "v_mfma_f32_16x16x32_bf16 %[c6], a[56:59], v[132:135], %[c6]\nv_mfma_f32_16x16x32_bf16 %[c7], a[60:63], v[132:135], %[c7]\ns_waitcnt lgkmcnt(2)\nv_mfma_f32_16x16x32_bf16 %[c0], a[64:67], v[136:139], %[c0]\n" \
  "ds_read_b128 v[128:131], %[al] offset:12288\nv_mfma_f32_16x16x32_bf16 %[c1], a[68:71], v[136:139], %[c1]\nds_read_b128 v[132:135], %[al] offset:13312\n" \
  "v_mfma_f32_16x16x32_bf16 %[c2], a[72:75], v[136:139], %[c2]\ns_add_u32 s90, s90, 4096\ns_addc_u32 s91, s91, 0\ns_mov_b32 m0, s92\ns_nop 0\nglobal_load_lds_dwordx4 %[l16], s[90:91] offset:0 sc1\n" \
  "v_mfma_f32_16x16x32_bf16 %[c3], a[76:79], v[136:139], %[c3]\nv_mfma_f32_16x16x32_bf16 %[c4], a[64:67], v[140:143], %[c4]\nv_mfma_f32_16x16x32_bf16 %[c5], a[68:71], v[140:143], %[c5]\n" \
  "v_mfma_f32_16x16x32_bf16 %[c6], a[72:75], v[140:143], %[c6]\nv_mfma_f32_16x16x32_bf16 %[c7], a[76:79], v[140:143], %[c7]\ns_waitcnt lgkmcnt(2)\nv_mfma_f32_16x16x32_bf16 %[c0], a[80:83], v[144:147], %[c0]\n" \
  "ds_read_b128 v[136:139], %[al] offset:14336\nv_mfma_f32_16x16x32_bf16 %[c1], a[84:87], v[144:147], %[c1]\nds_read_b128 v[140:143], %[al] offset:15360\n" \
  "v_mfma_f32_16x16x32_bf16 %[c2], a[88:91], v[144:147], %[c2]\nglobal_load_lds_dwordx4 %[l16], s[90:91] offset:1024 sc1\nv_mfma_f32_16x16x32_bf16 %[c3], a[92:95], v[144:147], %[c3]\n" \
  "v_mfma_f32_16x16x32_bf16 %[c4], a[80:83], v[148:151], %[c4]\nv_mfma_f32_16x16x32_bf16 %[c5], a[84:87], v[148:151], %[c5]\nv_mfma_f32_16x16x32_bf16 %[c6], a[88:91], v[148:151], %[c6]\n" \
  "v_mfma_f32_16x16x32_bf16 %[c7], a[92:95], v[148:151], %[c7]\ns_waitcnt lgkmcnt(2)\nv_mfma_f32_16x16x32_bf16 %[c0], a[96:99], v[128:131], %[c0]\nds_read_b128 v[144:147], %[al] offset:16384\n" \
  "v_mfma_f32_16x16x32_bf16 %[c1], a[100:103], v[128:131], %[c1]\nds_read_b128 v[148:151], %[al] offset:17408\nv_mfma_f32_16x16x32_bf16 %[c2], a[104:107], v[128:131], %[c2]\n" \
  "global_load_lds_dwordx4 %[l16], s[90:91] offset:2048 sc1\nv_mfma_f32_16x16x32_bf16 %[c3], a[108:111], v[128:131], %[c3]\nv_mfma_f32_16x16x32_bf16 %[c4], a[96:99], v[132:135], %[c4]\n" \
  "v_mfma_f32_16x16x32_bf16 %[c5], a[100:103], v[132:135], %[c5]\nv_mfma_f32_16x16x32_bf16 %[c6], a[104:107], v[132:135], %[c6]\nv_mfma_f32_16x16x32_bf16 %[c7], a[108:111], v[132:135], %[c7]\n" \
  "s_waitcnt lgkmcnt(2)\nv_mfma_f32_16x16x32_bf16 %[c0], a[112:115], v[136:139], %[c0]\nds_read_b128 v[128:131], %[al] offset:18432\nv_mfma_f32_16x16x32_bf16 %[c1], a[116:119], v[136:139], %[c1]\n" \
  "ds_read_b128 v[132:135], %[al] offset:19456\nv_mfma_f32_16x16x32_bf16 %[c2], a[120:123], v[136:139], %[c2]\nglobal_load_lds_dwordx4 %[l16], s[90:91] offset:3072 sc1\n" \
  "v_mfma_f32_16x16x32_bf16 %[c3], a[124:127], v[136:139], %[c3]\nv_mfma_f32_16x16x32_bf16 %[c4], a[112:115], v[140:143], %[c4]\nv_mfma_f32_16x16x32_bf16 %[c5], a[116:119], v[140:143], %[c5]\n" \
  "v_mfma_f32_16x16x32_bf16 %[c6], a[120:123], v[140:143], %[c6]\nv_mfma_f32_16x16x32_bf16 %[c7], a[124:127], v[140:143], %[c7]\ns_waitcnt lgkmcnt(2)\nv_mfma_f32_16x16x32_bf16 %[c0], a[128:131], v[144:147], %[c0]\n" \
  "ds_read_b128 v[136:139], %[al] offset:20480\nv_mfma_f32_16x16x32_bf16 %[c1], a[132:135], v[144:147], %[c1]\nds_read_b128 v[140:143], %[al] offset:21504\n" \
  "v_mfma_f32_16x16x32_bf16 %[c2], a[136:139], v[144:147], %[c2]\ns_mov_b32 m0, %[dst2]\ns_nop 0\nglobal_load_lds_dwordx4 %[l16], %[src2] offset:0 sc1\nv_mfma_f32_16x16x32_bf16 %[c3], a[140:143], v[144:147], %[c3]\n" \
  "v_mfma_f32_16x16x32_bf16 %[c4], a[128:131], v[148:151], %[c4]\nv_mfma_f32_16x16x32_bf16 %[c5], a[132:135], v[148:151], %[c5]\nv_mfma_f32_16x16x32_bf16 %[c6], a[136:139], v[148:151], %[c6]\n" \
  "v_mfma_f32_16x16x32_bf16 %[c7], a[140:143], v[148:151], %[c7]\ns_waitcnt lgkmcnt(2)\nv_mfma_f32_16x16x32_bf16 %[c0], a[144:147], v[128:131], %[c0]\nds_read_b128 v[144:147], %[al] offset:22528\n" \
  "v_mfma_f32_16x16x32_bf16 %[c1], a[148:151], v[128:131], %[c1]\nds_read_b128 v[148:151], %[al] offset:23552\nv_mfma_f32_16x16x32_bf16 %[c2], a[152:155], v[128:131], %[c2]\n" \
  "global_load_lds_dwordx4 %[l16], %[src2] offset:1024 sc1\nv_mfma_f32_16x16x32_bf16 %[c3], a[156:159], v[128:131], %[c3]\nv_mfma_f32_16x16x32_bf16 %[c4], a[144:147], v[132:135], %[c4]\n" \
  "v_mfma_f32_16x16x32_bf16 %[c5], a[148:151], v[132:135], %[c5]\nv_mfma_f32_16x16x32_bf16 %[c6], a[152:155], v[132:135], %[c6]\nv_mfma_f32_16x16x32_bf16 %[c7], a[156:159], v[132:135], %[c7]\n" \
  "s_waitcnt lgkmcnt(2)\nv_mfma_f32_16x16x32_bf16 %[c0], a[160:163], v[136:139], %[c0]\nds_read_b128 v[128:131], %[al] offset:24576\nv_mfma_f32_16x16x32_bf16 %[c1], a[164:167], v[136:139], %[c1]\n" \
  "ds_read_b128 v[132:135], %[al] offset:25600\nv_mfma_f32_16x16x32_bf16 %[c2], a[168:171], v[136:139], %[c2]\nv_mfma_f32_16x16x32_bf16 %[c3], a[172:175], v[136:139], %[c3]\n" \
  "v_mfma_f32_16x16x32_bf16 %[c4], a[160:163], v[140:143], %[c4]\nv_mfma_f32_16x16x32_bf16 %[c5], a[164:167], v[140:143], %[c5]\nv_mfma_f32_16x16x32_bf16 %[c6], a[168:171], v[140:143], %[c6]\n" \
  "v_mfma_f32_16x16x32_bf16 %[c7], a[172:175], v[140:143], %[c7]\ns_waitcnt lgkmcnt(2)\nv_mfma_f32_16x16x32_bf16 %[c0], a[176:179], v[144:147], %[c0]\nds_read_b128 v[136:139], %[al] offset:26624\n" \
  "v_mfma_f32_16x16x32_bf16 %[c1], a[180:183], v[144:147], %[c1]\nds_read_b128 v[140:143], %[al] offset:27648\nv_mfma_f32_16x16x32_bf16 %[c2], a[184:187], v[144:147], %[c2]\n" \
  "v_mov_b32 v184, %[fv]\ns_waitcnt vmcnt(10)\ns_mov_b64 exec, 1\nglobal_store_dword %[l16], v184, %[fl] sc1\ns_mov_b64 exec, -1\nv_mfma_f32_16x16x32_bf16 %[c3], a[188:191], v[144:147], %[c3]\n" \
  "v_mfma_f32_16x16x32_bf16 %[c4], a[176:179], v[148:151], %[c4]\nv_mfma_f32_16x16x32_bf16 %[c5], a[180:183], v[148:151], %[c5]\nv_mfma_f32_16x16x32_bf16 %[c6], a[184:187], v[148:151], %[c6]\n" \
  "v_mfma_f32_16x16x32_bf16 %[c7], a[188:191], v[148:151], %[c7]\ns_waitcnt lgkmcnt(2)\nv_mfma_f32_16x16x32_bf16 %[c0], a[192:195], v[128:131], %[c0]\nds_read_b128 v[144:147], %[al] offset:28672\n" \
  "v_mfma_f32_16x16x32_bf16 %[c1], a[196:199], v[128:131], %[c1]\nds_read_b128 v[148:151], %[al] offset:29696\nv_mfma_f32_16x16x32_bf16 %[c2], a[200:203], v[128:131], %[c2]\n" \
  "v_mfma_f32_16x16x32_bf16 %[c3], a[204:207], v[128:131], %[c3]\nv_mfma_f32_16x16x32_bf16 %[c4], a[192:195], v[132:135], %[c4]\nv_mfma_f32_16x16x32_bf16 %[c5], a[196:199], v[132:135], %[c5]\n" \
  "v_mfma_f32_16x16x32_bf16 %[c6], a[200:203], v[132:135], %[c6]\nv_mfma_f32_16x16x32_bf16 %[c7], a[204:207], v[132:135], %[c7]\ns_waitcnt lgkmcnt(2)\nv_mfma_f32_16x16x32_bf16 %[c0], a[208:211], v[136:139], %[c0]\n" \
  "ds_read_b128 v[128:131], %[al] offset:30720\nv_mfma_f32_16x16x32_bf16 %[c1], a[212:215], v[136:139], %[c1]\nds_read_b128 v[132:135], %[al] offset:31744\n" \
  "v_mfma_f32_16x16x32_bf16 %[c2], a[216:219], v[136:139], %[c2]\nds_read_b128 v[152:155], %[bl] offset:0\nv_mfma_f32_16x16x32_bf16 %[c3], a[220:223], v[136:139], %[c3]\n" \
  "ds_read_b128 v[156:159], %[bl] offset:1024\nv_mfma_f32_16x16x32_bf16 %[c4], a[208:211], v[140:143], %[c4]\nds_read_b128 v[160:163], %[bl] offset:2048\n" \
  "v_mfma_f32_16x16x32_bf16 %[c5], a[212:215], v[140:143], %[c5]\nds_read_b128 v[164:167], %[bl] offset:3072\nv_mfma_f32_16x16x32_bf16 %[c6], a[216:219], v[140:143], %[c6]\n" \
  "v_mfma_f32_16x16x32_bf16 %[c7], a[220:223], v[140:143], %[c7]\ns_waitcnt lgkmcnt(6)\nv_mfma_f32_16x16x32_bf16 %[c0], a[224:227], v[144:147], %[c0]\nds_read_b128 v[136:139], %[al] offset:32768\n" \
  "v_mfma_f32_16x16x32_bf16 %[c1], a[228:231], v[144:147], %[c1]\nds_read_b128 v[140:143], %[al] offset:33792\nv_mfma_f32_16x16x32_bf16 %[c2], a[232:235], v[144:147], %[c2]\n" \
  "ds_read_b128 v[168:171], %[bl] offset:4096\nv_mfma_f32_16x16x32_bf16 %[c3], a[236:239], v[144:147], %[c3]\nds_read_b128 v[172:175], %[bl] offset:5120\n" \
  "v_mfma_f32_16x16x32_bf16 %[c4], a[224:227], v[148:151], %[c4]\nds_read_b128 v[176:179], %[bl] offset:6144\nv_mfma_f32_16x16x32_bf16 %[c5], a[228:231], v[148:151], %[c5]\n" \
  "ds_read_b128 v[180:183], %[bl] offset:7168\nv_mfma_f32_16x16x32_bf16 %[c6], a[232:235], v[148:151], %[c6]\nv_mfma_f32_16x16x32_bf16 %[c7], a[236:239], v[148:151], %[c7]\n" \
  "s_waitcnt lgkmcnt(10)\nv_mfma_f32_16x16x32_bf16 %[c0], a[240:243], v[128:131], %[c0]\nds_read_b128 v[144:147], %[al] offset:34816\nv_mfma_f32_16x16x32_bf16 %[c1], a[244:247], v[128:131], %[c1]\n" \
  "ds_read_b128 v[148:151], %[al] offset:35840\nv_mfma_f32_16x16x32_bf16 %[c2], a[248:251], v[128:131], %[c2]\nv_mfma_f32_16x16x32_bf16 %[c3], a[252:255], v[128:131], %[c3]\n" \
  "v_mfma_f32_16x16x32_bf16 %[c4], a[240:243], v[132:135], %[c4]\nv_mfma_f32_16x16x32_bf16 %[c5], a[244:247], v[132:135], %[c5]\nv_mfma_f32_16x16x32_bf16 %[c6], a[248:251], v[132:135], %[c6]\n" \
  "v_mfma_f32_16x16x32_bf16 %[c7], a[252:255], v[132:135], %[c7]\ns_waitcnt lgkmcnt(6)\nv_mfma_f32_16x16x32_bf16 %[c0], v[152:155], v[136:139], %[c0]\nds_read_b128 v[128:131], %[al] offset:36864\n" \
  "v_mfma_f32_16x16x32_bf16 %[c1], v[156:159], v[136:139], %[c1]\nds_read_b128 v[132:135], %[al] offset:37888\nv_mfma_f32_16x16x32_bf16 %[c2], v[160:163], v[136:139], %[c2]\n" \
  "v_mfma_f32_16x16x32_bf16 %[c3], v[164:167], v[136:139], %[c3]\nv_mfma_f32_16x16x32_bf16 %[c4], v[152:155], v[140:143], %[c4]\nds_read_b128 v[152:155], %[bl] offset:8192\n" \
  "v_mfma_f32_16x16x32_bf16 %[c5], v[156:159], v[140:143], %[c5]\nds_read_b128 v[156:159], %[bl] offset:9216\nv_mfma_f32_16x16x32_bf16 %[c6], v[160:163], v[140:143], %[c6]\n" \
  "ds_read_b128 v[160:163], %[bl] offset:10240\nv_mfma_f32_16x16x32_bf16 %[c7], v[164:167], v[140:143], %[c7]\nds_read_b128 v[164:167], %[bl] offset:11264\n" \
  "s_waitcnt lgkmcnt(6)\nv_mfma_f32_16x16x32_bf16 %[c0], v[168:171], v[144:147], %[c0]\nds_read_b128 v[136:139], %[al] offset:38912\nv_mfma_f32_16x16x32_bf16 %[c1], v[172:175], v[144:147], %[c1]\n" \
  "ds_read_b128 v[140:143], %[al] offset:39936\nv_mfma_f32_16x16x32_bf16 %[c2], v[176:179], v[144:147], %[c2]\nv_mfma_f32_16x16x32_bf16 %[c3], v[180:183], v[144:147], %[c3]\n" \
  "v_mfma_f32_16x16x32_bf16 %[c4], v[168:171], v[148:151], %[c4]\nds_read_b128 v[168:171], %[bl] offset:12288\nv_mfma_f32_16x16x32_bf16 %[c5], v[172:175], v[148:151], %[c5]\n" \
  "ds_read_b128 v[172:175], %[bl] offset:13312\nv_mfma_f32_16x16x32_bf16 %[c6], v[176:179], v[148:151], %[c6]\nds_read_b128 v[176:179], %[bl] offset:14336\n" \
  "v_mfma_f32_16x16x32_bf16 %[c7], v[180:183], v[148:151], %[c7]\nds_read_b128 v[180:183], %[bl] offset:15360\ns_waitcnt lgkmcnt(6)\nv_mfma_f32_16x16x32_bf16 %[c0], v[152:155], v[128:131], %[c0]\n" \
  "v_mfma_f32_16x16x32_bf16 %[c1], v[156:159], v[128:131], %[c1]\nv_mfma_f32_16x16x32_bf16 %[c2], v[160:163], v[128:131], %[c2]\nv_mfma_f32_16x16x32_bf16 %[c3], v[164:167], v[128:131], %[c3]\n" \
  "v_mfma_f32_16x16x32_bf16 %[c4], v[152:155], v[132:135], %[c4]\nv_mfma_f32_16x16x32_bf16 %[c5], v[156:159], v[132:135], %[c5]\nv_mfma_f32_16x16x32_bf16 %[c6], v[160:163], v[132:135], %[c6]\n" \
  "v_mfma_f32_16x16x32_bf16 %[c7], v[164:167], v[132:135], %[c7]\ns_waitcnt lgkmcnt(0)\nv_mfma_f32_16x16x32_bf16 %[c0], v[168:171], v[136:139], %[c0]\nv_mfma_f32_16x16x32_bf16 %[c1], v[172:175], v[136:139], %[c1]\n" \
  "v_mfma_f32_16x16x32_bf16 %[c2], v[176:179], v[136:139], %[c2]\nv_mfma_f32_16x16x32_bf16 %[c3], v[180:183], v[136:139], %[c3]\nv_mfma_f32_16x16x32_bf16 %[c4], v[168:171], v[140:143], %[c4]\n" \
  "v_mfma_f32_16x16x32_bf16 %[c5], v[172:175], v[140:143], %[c5]\nv_mfma_f32_16x16x32_bf16 %[c6], v[176:179], v[140:143], %[c6]\nv_mfma_f32_16x16x32_bf16 %[c7], v[180:183], v[140:143], %[c7]\n" \
  "s_nop 7\n"
#define R1_MAIN_X_ASM \
  "ds_read_b128 v[128:131], %[al] offset:0\nds_read_b128 v[132:135], %[al] offset:1024\nds_read_b128 v[136:139], %[al] offset:2048\nds_read_b128 v[140:143], %[al] offset:3072\n" \
  "s_waitcnt lgkmcnt(2)\nv_mfma_f32_16x16x32_bf16 %[c0], v[128:131], a[0:3], 0\nds_read_b128 v[144:147], %[al] offset:4096\nv_mfma_f32_16x16x32_bf16 %[c1], v[128:131], a[4:7], 0\n" \
  "ds_read_b128 v[148:151], %[al] offset:5120\nv_mfma_f32_16x16x32_bf16 %[c2], v[128:131], a[8:11], 0\ns_mov_b64 s[90:91], %[src]\ns_mov_b32 m0, %[dst]\ns_add_u32 s92, %[dst], 4096\n" \
  "global_load_lds_dwordx4 %[l16], s[90:91] sc1\nv_mfma_f32_16x16x32_bf16 %[c3], v[128:131], a[12:15], 0\nv_mfma_f32_16x16x32_bf16 %[c4], v[132:135], a[0:3], 0\n" \
  "v_mfma_f32_16x16x32_bf16 %[c5], v[132:135], a[4:7], 0\nv_mfma_f32_16x16x32_bf16 %[c6], v[132:135], a[8:11], 0\nv_mfma_f32_16x16x32_bf16 %[c7], v[132:135], a[12:15], 0\n" \
  "s_waitcnt lgkmcnt(2)\nv_mfma_f32_16x16x32_bf16 %[c0], v[136:139], a[16:19], %[c0]\nds_read_b128 v[128:131], %[al] offset:6144\nv_mfma_f32_16x16x32_bf16 %[c1], v[136:139], a[20:23], %[c1]\n" \
  "ds_read_b128 v[132:135], %[al] offset:7168\nv_mfma_f32_16x16x32_bf16 %[c2], v[136:139], a[24:27], %[c2]\nglobal_load_lds_dwordx4 %[l16], s[90:91] offset:1024 sc1\n" \
  "v_mfma_f32_16x16x32_bf16 %[c3], v[136:139], a[28:31], %[c3]\nv_mfma_f32_16x16x32_bf16 %[c4], v[140:143], a[16:19], %[c4]\nv_mfma_f32_16x16x32_bf16 %[c5], v[140:143], a[20:23], %[c5]\n" \
  "v_mfma_f32_16x16x32_bf16 %[c6], v[140:143], a[24:27], %[c6]\nv_mfma_f32_16x16x32_bf16 %[c7], v[140:143], a[28:31], %[c7]\ns_waitcnt lgkmcnt(2)\nv_mfma_f32_16x16x32_bf16 %[c0], v[144:147], a[32:35], %[c0]\n" \
  "ds_read_b128 v[136:139], %[al] offset:8192\nv_mfma_f32_16x16x32_bf16 %[c1], v[144:147], a[36:39], %[c1]\nds_read_b128 v[140:143], %[al] offset:9216\nv_mfma_f32_16x16x32_bf16 %[c2], v[144:147], a[40:43], %[c2]\n" \
  "global_load_lds_dwordx4 %[l16], s[90:91] offset:2048 sc1\nv_mfma_f32_16x16x32_bf16 %[c3], v[144:147], a[44:47], %[c3]\nv_mfma_f32_16x16x32_bf16 %[c4], v[148:151], a[32:35], %[c4]\n" \
  "v_mfma_f32_16x16x32_bf16 %[c5], v[148:151], a[36:39], %[c5]\nv_mfma_f32_16x16x32_bf16 %[c6], v[148:151], a[40:43], %[c6]\nv_mfma_f32_16x16x32_bf16 %[c7], v[148:151], a[44:47], %[c7]\n" \
  "s_waitcnt lgkmcnt(2)\nv_mfma_f32_16x16x32_bf16 %[c0], v[128:131], a[48:51], %[c0]\nds_read_b128 v[144:147], %[al] offset:10240\nv_mfma_f32_16x16x32_bf16 %[c1], v[128:131], a[52:55], %[c1]\n" \
  "ds_read_b128 v[148:151], %[al] offset:11264\nv_mfma_f32_16x16x32_bf16 %[c2], v[128:131], a[56:59], %[c2]\nglobal_load_lds_dwordx4 %[l16], s[90:91] offset:3072 sc1\n" \
  "v_mfma_f32_16x16x32_bf16 %[c3], v[128:131], a[60:63], %[c3]\nv_mfma_f32_16x16x32_bf16 %[c4], v[132:135], a[48:51], %[c4]\nv_mfma_f32_16x16x32_bf16 %[c5], v[132:135], a[52:55], %[c5]\n" \
  "v_mfma_f32_16x16x32_bf16 %[c6], v[132:135], a[56:59], %[c6]\nv_mfma_f32_16x16x32_bf16 %[c7], v[132:135], a[60:63], %[c7]\ns_waitcnt lgkmcnt(2)\nv_mfma_f32_16x16x32_bf16 %[c0], v[136:139], a[64:67], %[c0]\n" \
  "ds_read_b128 v[128:131], %[al] offset:12288\nv_mfma_f32_16x16x32_bf16 %[c1], v[136:139], a[68:71], %[c1]\nds_read_b128 v[132:135], %[al] offset:13312\n" \
  "v_mfma_f32_16x16x32_bf16 %[c2], v[136:139], a[72:75], %[c2]\ns_add_u32 s90, s90, 4096\ns_addc_u32 s91, s91, 0\ns_mov_b32 m0, s92\ns_nop 0\nglobal_load_lds_dwordx4 %[l16], s[90:91] offset:0 sc1\n" \
  "v_mfma_f32_16x16x32_bf16 %[c3], v[136:139], a[76:79], %[c3]\nv_mfma_f32_16x16x32_bf16 %[c4], v[140:143], a[64:67], %[c4]\nv_mfma_f32_16x16x32_bf16 %[c5], v[140:143], a[68:71], %[c5]\n" \
  "v_mfma_f32_16x16x32_bf16 %[c6], v[140:143], a[72:75], %[c6]\nv_mfma_f32_16x16x32_bf16 %[c7], v[140:143], a[76:79], %[c7]\ns_waitcnt lgkmcnt(2)\nv_mfma_f32_16x16x32_bf16 %[c0], v[144:147], a[80:83], %[c0]\n" \
  "ds_read_b128 v[136:139], %[al] offset:14336\nv_mfma_f32_16x16x32_bf16 %[c1], v[144:147], a[84:87], %[c1]\nds_read_b128 v[140:143], %[al] offset:15360\n" \
  "v_mfma_f32_16x16x32_bf16 %[c2], v[144:147], a[88:91], %[c2]\nglobal_load_lds_dwordx4 %[l16], s[90:91] offset:1024 sc1\nv_mfma_f32_16x16x32_bf16 %[c3], v[144:147], a[92:95], %[c3]\n" \
  "v_mfma_f32_16x16x32_bf16 %[c4], v[148:151], a[80:83], %[c4]\nv_mfma_f32_16x16x32_bf16 %[c5], v[148:151], a[84:87], %[c5]\nv_mfma_f32_16x16x32_bf16 %[c6], v[148:151], a[88:91], %[c6]\n" \
  "v_mfma_f32_16x16x32_bf16 %[c7], v[148:151], a[92:95], %[c7]\ns_waitcnt lgkmcnt(2)\nv_mfma_f32_16x16x32_bf16 %[c0], v[128:131], a[96:99], %[c0]\nds_read_b128 v[144:147], %[al] offset:16384\n" \
  "v_mfma_f32_16x16x32_bf16 %[c1], v[128:131], a[100:103], %[c1]\nds_read_b128 v[148:151], %[al] offset:17408\nv_mfma_f32_16x16x32_bf16 %[c2], v[128:131], a[104:107], %[c2]\n" \
  "global_load_lds_dwordx4 %[l16], s[90:91] offset:2048 sc1\nv_mfma_f32_16x16x32_bf16 %[c3], v[128:131], a[108:111], %[c3]\nv_mfma_f32_16x16x32_bf16 %[c4], v[132:135], a[96:99], %[c4]\n" \
  "v_mfma_f32_16x16x32_bf16 %[c5], v[132:135], a[100:103], %[c5]\nv_mfma_f32_16x16x32_bf16 %[c6], v[132:135], a[104:107], %[c6]\nv_mfma_f32_16x16x32_bf16 %[c7], v[132:135], a[108:111], %[c7]\n" \
  "s_waitcnt lgkmcnt(2)\nv_mfma_f32_16x16x32_bf16 %[c0], v[136:139], a[112:115], %[c0]\nds_read_b128 v[128:131], %[al] offset:18432\nv_mfma_f32_16x16x32_bf16 %[c1], v[136:139], a[116:119], %[c1]\n" \
  "ds_read_b128 v[132:135], %[al] offset:19456\nv_mfma_f32_16x16x32_bf16 %[c2], v[136:139], a[120:123], %[c2]\nglobal_load_lds_dwordx4 %[l16], s[90:91] offset:3072 sc1\n" \
  "v_mfma_f32_16x16x32_bf16 %[c3], v[136:139], a[124:127], %[c3]\nv_mfma_f32_16x16x32_bf16 %[c4], v[140:143], a[112:115], %[c4]\nv_mfma_f32_16x16x32_bf16 %[c5], v[140:143], a[116:119], %[c5]\n" \
  "v_mfma_f32_16x16x32_bf16 %[c6], v[140:143], a[120:123], %[c6]\nv_mfma_f32_16x16x32_bf16 %[c7], v[140:143], a[124:127], %[c7]\ns_waitcnt lgkmcnt(2)\nv_mfma_f32_16x16x32_bf16 %[c0], v[144:147], a[128:131], %[c0]\n" \
  "ds_read_b128 v[136:139], %[al] offset:20480\nv_mfma_f32_16x16x32_bf16 %[c1], v[144:147], a[132:135], %[c1]\nds_read_b128 v[140:143], %[al] offset:21504\n" \
  "v_mfma_f32_16x16x32_bf16 %[c2], v[144:147], a[136:139], %[c2]\ns_mov_b32 m0, %[dst2]\ns_nop 0\nglobal_load_lds_dwordx4 %[l16], %[src2] offset:0 sc1\nv_mfma_f32_16x16x32_bf16 %[c3], v[144:147], a[140:143], %[c3]\n" \
  "v_mfma_f32_16x16x32_bf16 %[c4], v[148:151], a[128:131], %[c4]\nv_mfma_f32_16x16x32_bf16 %[c5], v[148:151], a[132:135], %[c5]\nv_mfma_f32_16x16x32_bf16 %[c6], v[148:151], a[136:139], %[c6]\n" \
  "v_mfma_f32_16x16x32_bf16 %[c7], v[148:151], a[140:143], %[c7]\ns_waitcnt lgkmcnt(2)\nv_mfma_f32_16x16x32_bf16 %[c0], v[128:131], a[144:147], %[c0]\nds_read_b128 v[144:147], %[al] offset:22528\n" \
  "v_mfma_f32_16x16x32_bf16 %[c1], v[128:131], a[148:151], %[c1]\nds_read_b128 v[148:151], %[al] offset:23552\nv_mfma_f32_16x16x32_bf16 %[c2], v[128:131], a[152:155], %[c2]\n" \
  "global_load_lds_dwordx4 %[l16], %[src2] offset:1024 sc1\nv_mfma_f32_16x16x32_bf16 %[c3], v[128:131], a[156:159], %[c3]\nv_mfma_f32_16x16x32_bf16 %[c4], v[132:135], a[144:147], %[c4]\n" \
  "v_mfma_f32_16x16x32_bf16 %[c5], v[132:135], a[148:151], %[c5]\nv_mfma_f32_16x16x32_bf16 %[c6], v[132:135], a[152:155], %[c6]\nv_mfma_f32_16x16x32_bf16 %[c7], v[132:135], a[156:159], %[c7]\n" \
  "s_waitcnt lgkmcnt(2)\nv_mfma_f32_16x16x32_bf16 %[c0], v[136:139], a[160:163], %[c0]\nds_read_b128 v[128:131], %[al] offset:24576\nv_mfma_f32_16x16x32_bf16 %[c1], v[136:139], a[164:167], %[c1]\n" \
  "ds_read_b128 v[132:135], %[al] offset:25600\nv_mfma_f32_16x16x32_bf16 %[c2], v[136:139], a[168:171], %[c2]\nv_mfma_f32_16x16x32_bf16 %[c3], v[136:139], a[172:175], %[c3]\n" \
  "v_mfma_f32_16x16x32_bf16 %[c4], v[140:143], a[160:163], %[c4]\nv_mfma_f32_16x16x32_bf16 %[c5], v[140:143], a[164:167], %[c5]\nv_mfma_f32_16x16x32_bf16 %[c6], v[140:143], a[168:171], %[c6]\n" \
  "v_mfma_f32_16x16x32_bf16 %[c7], v[140:143], a[172:175], %[c7]\ns_waitcnt lgkmcnt(2)\nv_mfma_f32_16x16x32_bf16 %[c0], v[144:147], a[176:179], %[c0]\nds_read_b128 v[136:139], %[al] offset:26624\n" \
  "v_mfma_f32_16x16x32_bf16 %[c1], v[144:147], a[180:183], %[c1]\nds_read_b128 v[140:143], %[al] offset:27648\nv_mfma_f32_16x16x32_bf16 %[c2], v[144:147], a[184:187], %[c2]\n" \
  "v_mov_b32 v184, %[fv]\ns_waitcnt vmcnt(10)\ns_mov_b64 exec, 1\nglobal_store_dword %[l16], v184, %[fl] sc1\ns_mov_b64 exec, -1\nv_mfma_f32_16x16x32_bf16 %[c3], v[144:147], a[188:191], %[c3]\n" \
  "v_mfma_f32_16x16x32_bf16 %[c4], v[148:151], a[176:179], %[c4]\nv_mfma_f32_16x16x32_bf16 %[c5], v[148:151], a[180:183], %[c5]\nv_mfma_f32_16x16x32_bf16 %[c6], v[148:151], a[184:187], %[c6]\n" \
  "v_mfma_f32_16x16x32_bf16 %[c7], v[148:151], a[188:191], %[c7]\ns_waitcnt lgkmcnt(2)\nv_mfma_f32_16x16x32_bf16 %[c0], v[128:131], a[192:195], %[c0]\nds_read_b128 v[144:147], %[al] offset:28672\n" \
  "v_mfma_f32_16x16x32_bf16 %[c1], v[128:131], a[196:199], %[c1]\nds_read_b128 v[148:151], %[al] offset:29696\nv_mfma_f32_16x16x32_bf16 %[c2], v[128:131], a[200:203], %[c2]\n" \
  "v_mfma_f32_16x16x32_bf16 %[c3], v[128:131], a[204:207], %[c3]\nv_mfma_f32_16x16x32_bf16 %[c4], v[132:135], a[192:195], %[c4]\nv_mfma_f32_16x16x32_bf16 %[c5], v[132:135], a[196:199], %[c5]\n" \
  "v_mfma_f32_16x16x32_bf16 %[c6], v[132:135], a[200:203], %[c6]\nv_mfma_f32_16x16x32_bf16 %[c7], v[132:135], a[204:207], %[c7]\ns_waitcnt lgkmcnt(2)\nv_mfma_f32_16x16x32_bf16 %[c0], v[136:139], a[208:211], %[c0]\n" \
  "ds_read_b128 v[128:131], %[al] offset:30720\nv_mfma_f32_16x16x32_bf16 %[c1], v[136:139], a[212:215], %[c1]\nds_read_b128 v[132:135], %[al] offset:31744\n" \
  "v_mfma_f32_16x16x32_bf16 %[c2], v[136:139], a[216:219], %[c2]\nds_read_b128 v[152:155], %[bl] offset:0\nv_mfma_f32_16x16x32_bf16 %[c3], v[136:139], a[220:223], %[c3]\n" \
  "ds_read_b128 v[156:159], %[bl] offset:1024\nv_mfma_f32_16x16x32_bf16 %[c4], v[140:143], a[208:211], %[c4]\nds_read_b128 v[160:163], %[bl] offset:2048\n" \
  "v_mfma_f32_16x16x32_bf16 %[c5], v[140:143], a[212:215], %[c5]\nds_read_b128 v[164:167], %[bl] offset:3072\nv_mfma_f32_16x16x32_bf16 %[c6], v[140:143], a[216:219], %[c6]\n" \
  "v_mfma_f32_16x16x32_bf16 %[c7], v[140:143], a[220:223], %[c7]\ns_waitcnt lgkmcnt(6)\nv_mfma_f32_16x16x32_bf16 %[c0], v[144:147], a[224:227], %[c0]\nds_read_b128 v[136:139], %[al] offset:32768\n" \
  "v_mfma_f32_16x16x32_bf16 %[c1], v[144:147], a[228:231], %[c1]\nds_read_b128 v[140:143], %[al] offset:33792\nv_mfma_f32_16x16x32_bf16 %[c2], v[144:147], a[232:235], %[c2]\n" \
  "ds_read_b128 v[168:171], %[bl] offset:4096\nv_mfma_f32_16x16x32_bf16 %[c3], v[144:147], a[236:239], %[c3]\nds_read_b128 v[172:175], %[bl] offset:5120\n" \
  "v_mfma_f32_16x16x32_bf16 %[c4], v[148:151], a[224:227], %[c4]\nds_read_b128 v[176:179], %[bl] offset:6144\nv_mfma_f32_16x16x32_bf16 %[c5], v[148:151], a[228:231], %[c5]\n" \
  "ds_read_b128 v[180:183], %[bl] offset:7168\nv_mfma_f32_16x16x32_bf16 %[c6], v[148:151], a[232:235], %[c6]\nv_mfma_f32_16x16x32_bf16 %[c7], v[148:151], a[236:239], %[c7]\n" \
  "s_waitcnt lgkmcnt(10)\nv_mfma_f32_16x16x32_bf16 %[c0], v[128:131], a[240:243], %[c0]\nds_read_b128 v[144:147], %[al] offset:34816\nv_mfma_f32_16x16x32_bf16 %[c1], v[128:131], a[244:247], %[c1]\n" \
  "ds_read_b128 v[148:151], %[al] offset:35840\nv_mfma_f32_16x16x32_bf16 %[c2], v[128:131], a[248:251], %[c2]\nv_mfma_f32_16x16x32_bf16 %[c3], v[128:131], a[252:255], %[c3]\n" \
  "v_mfma_f32_16x16x32_bf16 %[c4], v[132:135], a[240:243], %[c4]\nv_mfma_f32_16x16x32_bf16 %[c5], v[132:135], a[244:247], %[c5]\nv_mfma_f32_16x16x32_bf16 %[c6], v[132:135], a[248:251], %[c6]\n" \
  "v_mfma_f32_16x16x32_bf16 %[c7], v[132:135], a[252:255], %[c7]\ns_waitcnt lgkmcnt(6)\nv_mfma_f32_16x16x32_bf16 %[c0], v[136:139], v[152:155], %[c0]\nds_read_b128 v[128:131], %[al] offset:36864\n" \
  "v_mfma_f32_16x16x32_bf16 %[c1], v[136:139], v[156:159], %[c1]\nds_read_b128 v[132:135], %[al] offset:37888\nv_mfma_f32_16x16x32_bf16 %[c2], v[136:139], v[160:163], %[c2]\n" \
  "v_mfma_f32_16x16x32_bf16 %[c3], v[136:139], v[164:167], %[c3]\nv_mfma_f32_16x16x32_bf16 %[c4], v[140:143], v[152:155], %[c4]\nds_read_b128 v[152:155], %[bl] offset:8192\n" \
  "v_mfma_f32_16x16x32_bf16 %[c5], v[140:143], v[156:159], %[c5]\nds_read_b128 v[156:159], %[bl] offset:9216\nv_mfma_f32_16x16x32_bf16 %[c6], v[140:143], v[160:163], %[c6]\n" \
  "ds_read_b128 v[160:163], %[bl] offset:10240\nv_mfma_f32_16x16x32_bf16 %[c7], v[140:143], v[164:167], %[c7]\nds_read_b128 v[164:167], %[bl] offset:11264\n" \
  "s_waitcnt lgkmcnt(6)\nv_mfma_f32_16x16x32_bf16 %[c0], v[144:147], v[168:171], %[c0]\nds_read_b128 v[136:139], %[al] offset:38912\nv_mfma_f32_16x16x32_bf16 %[c1], v[144:147], v[172:175], %[c1]\n" \
  "ds_read_b128 v[140:143], %[al] offset:39936\nv_mfma_f32_16x16x32_bf16 %[c2], v[144:147], v[176:179], %[c2]\nv_mfma_f32_16x16x32_bf16 %[c3], v[144:147], v[180:183], %[c3]\n" \
  "v_mfma_f32_16x16x32_bf16 %[c4], v[148:151], v[168:171], %[c4]\nds_read_b128 v[168:171], %[bl] offset:12288\nv_mfma_f32_16x16x32_bf16 %[c5], v[148:151], v[172:175], %[c5]\n" \
  "ds_read_b128 v[172:175], %[bl] offset:13312\nv_mfma_f32_16x16x32_bf16 %[c6], v[148:151], v[176:179], %[c6]\nds_read_b128 v[176:179], %[bl] offset:14336\n" \
  "v_mfma_f32_16x16x32_bf16 %[c7], v[148:151], v[180:183], %[c7]\nds_read_b128 v[180:183], %[bl] offset:15360\ns_waitcnt lgkmcnt(6)\nv_mfma_f32_16x16x32_bf16 %[c0], v[128:131], v[152:155], %[c0]\n" \
  "v_mfma_f32_16x16x32_bf16 %[c1], v[128:131], v[156:159], %[c1]\nv_mfma_f32_16x16x32_bf16 %[c2], v[128:131], v[160:163], %[c2]\nv_mfma_f32_16x16x32_bf16 %[c3], v[128:131], v[164:167], %[c3]\n" \
  "v_mfma_f32_16x16x32_bf16 %[c4], v[132:135], v[152:155], %[c4]\nv_mfma_f32_16x16x32_bf16 %[c5], v[132:135], v[156:159], %[c5]\nv_mfma_f32_16x16x32_bf16 %[c6], v[132:135], v[160:163], %[c6]\n" \
  "v_mfma_f32_16x16x32_bf16 %[c7], v[132:135], v[164:167], %[c7]\ns_waitcnt lgkmcnt(0)\nv_mfma_f32_16x16x32_bf16 %[c0], v[136:139], v[168:171], %[c0]\nv_mfma_f32_16x16x32_bf16 %[c1], v[136:139], v[172:175], %[c1]\n" \
  "v_mfma_f32_16x16x32_bf16 %[c2], v[136:139], v[176:179], %[c2]\nv_mfma_f32_16x16x32_bf16 %[c3], v[136:139], v[180:183], %[c3]\nv_mfma_f32_16x16x32_bf16 %[c4], v[140:143], v[168:171], %[c4]\n" \
  "v_mfma_f32_16x16x32_bf16 %[c5], v[140:143], v[172:175], %[c5]\nv_mfma_f32_16x16x32_bf16 %[c6], v[140:143], v[176:179], %[c6]\nv_mfma_f32_16x16x32_bf16 %[c7], v[140:143], v[180:183], %[c7]\n" \
  "s_nop 7\n"
#define R1_MAIN_CLOB \
  "v128","v129","v130","v131","v132","v133","v134","v135","v136","v137","v138","v139","v140","v141","v142","v143","v144","v145","v146","v147","v148","v149", \
  "v150","v151","v152","v153","v154","v155","v156","v157","v158","v159","v160","v161","v162","v163","v164","v165","v166","v167","v168","v169","v170","v171", \
  "v172","v173","v174","v175","v176","v177","v178","v179","v180","v181","v182","v183","v184","s90","s91","s92","s93"
#define R2_LOAD_ASM \
  "s_mov_b32 s93, %[lw]\ns_min_u32 s92, %[rmax], 0\ns_mul_i32 s92, s92, %[rs]\ns_mov_b32 m0, s93\ns_add_u32 s90, %[wlo], s92\ns_addc_u32 s91, %[whi], 0\nglobal_load_lds_dwordx4 %[vs], s[90:91]\n" \
  "s_add_u32 s93, s93, 1024\ns_min_u32 s92, %[rmax], 8\ns_mul_i32 s92, s92, %[rs]\ns_mov_b32 m0, s93\ns_add_u32 s90, %[wlo], s92\ns_addc_u32 s91, %[whi], 0\n" \
  "global_load_lds_dwordx4 %[vs], s[90:91]\ns_add_u32 s93, s93, 1024\ns_min_u32 s92, %[rmax], 16\ns_mul_i32 s92, s92, %[rs]\ns_mov_b32 m0, s93\ns_add_u32 s90, %[wlo], s92\n" \
  "s_addc_u32 s91, %[whi], 0\nglobal_load_lds_dwordx4 %[vs], s[90:91]\ns_add_u32 s93, s93, 1024\ns_min_u32 s92, %[rmax], 24\ns_mul_i32 s92, s92, %[rs]\ns_mov_b32 m0, s93\n" \
  "s_add_u32 s90, %[wlo], s92\ns_addc_u32 s91, %[whi], 0\nglobal_load_lds_dwordx4 %[vs], s[90:91]\ns_add_u32 s93, s93, 1024\ns_min_u32 s92, %[rmax], 32\ns_mul_i32 s92, s92, %[rs]\n" \
  "s_mov_b32 m0, s93\ns_add_u32 s90, %[wlo], s92\ns_addc_u32 s91, %[whi], 0\nglobal_load_lds_dwordx4 %[vs], s[90:91]\ns_add_u32 s93, s93, 1024\ns_min_u32 s92, %[rmax], 40\n" \
  "s_mul_i32 s92, s92, %[rs]\ns_mov_b32 m0, s93\ns_add_u32 s90, %[wlo], s92\ns_addc_u32 s91, %[whi], 0\nglobal_load_lds_dwordx4 %[vs], s[90:91]\ns_add_u32 s93, s93, 1024\n" \
  "s_min_u32 s92, %[rmax], 48\ns_mul_i32 s92, s92, %[rs]\ns_mov_b32 m0, s93\ns_add_u32 s90, %[wlo], s92\ns_addc_u32 s91, %[whi], 0\nglobal_load_lds_dwordx4 %[vs], s[90:91]\n" \
  "s_add_u32 s93, s93, 1024\ns_min_u32 s92, %[rmax], 56\ns_mul_i32 s92, s92, %[rs]\ns_mov_b32 m0, s93\ns_add_u32 s90, %[wlo], s92\ns_addc_u32 s91, %[whi], 0\n" \
  "global_load_lds_dwordx4 %[vs], s[90:91]\ns_add_u32 s93, s93, 1024\ns_min_u32 s92, %[rmax], 64\ns_mul_i32 s92, s92, %[rs]\ns_mov_b32 m0, s93\ns_add_u32 s90, %[wlo], s92\n" \
  "s_addc_u32 s91, %[whi], 0\nglobal_load_lds_dwordx4 %[vs], s[90:91]\ns_add_u32 s93, s93, 1024\ns_min_u32 s92, %[rmax], 72\ns_mul_i32 s92, s92, %[rs]\ns_mov_b32 m0, s93\n" \
  "s_add_u32 s90, %[wlo], s92\ns_addc_u32 s91, %[whi], 0\nglobal_load_lds_dwordx4 %[vs], s[90:91]\ns_add_u32 s93, s93, 1024\ns_min_u32 s92, %[rmax], 80\ns_mul_i32 s92, s92, %[rs]\n" \
  "s_mov_b32 m0, s93\ns_add_u32 s90, %[wlo], s92\ns_addc_u32 s91, %[whi], 0\nglobal_load_lds_dwordx4 %[vs], s[90:91]\ns_add_u32 s93, s93, 1024\ns_min_u32 s92, %[rmax], 88\n" \
  "s_mul_i32 s92, s92, %[rs]\ns_mov_b32 m0, s93\ns_add_u32 s90, %[wlo], s92\ns_addc_u32 s91, %[whi], 0\nglobal_load_lds_dwordx4 %[vs], s[90:91]\ns_add_u32 s93, s93, 1024\n" \
  "s_min_u32 s92, %[rmax], 96\ns_mul_i32 s92, s92, %[rs]\ns_mov_b32 m0, s93\ns_add_u32 s90, %[wlo], s92\ns_addc_u32 s91, %[whi], 0\nglobal_load_lds_dwordx4 %[vs], s[90:91]\n" \
  "s_add_u32 s93, s93, 1024\ns_min_u32 s92, %[rmax], 104\ns_mul_i32 s92, s92, %[rs]\ns_mov_b32 m0, s93\ns_add_u32 s90, %[wlo], s92\ns_addc_u32 s91, %[whi], 0\n" \
  "global_load_lds_dwordx4 %[vs], s[90:91]\ns_add_u32 s93, s93, 1024\ns_min_u32 s92, %[rmax], 112\ns_mul_i32 s92, s92, %[rs]\ns_mov_b32 m0, s93\ns_add_u32 s90, %[wlo], s92\n" \
  "s_addc_u32 s91, %[whi], 0\nglobal_load_lds_dwordx4 %[vs], s[90:91]\ns_add_u32 s93, s93, 1024\ns_min_u32 s92, %[rmax], 120\ns_mul_i32 s92, s92, %[rs]\ns_mov_b32 m0, s93\n" \
  "s_add_u32 s90, %[wlo], s92\ns_addc_u32 s91, %[whi], 0\nglobal_load_lds_dwordx4 %[vs], s[90:91]\ns_add_u32 s93, s93, 1024\ns_min_u32 s92, %[rmax], 128\n" \
  "s_mul_i32 s92, s92, %[rs]\ns_mov_b32 m0, s93\ns_add_u32 s90, %[wlo], s92\ns_addc_u32 s91, %[whi], 0\nglobal_load_lds_dwordx4 %[vs], s[90:91]\ns_add_u32 s93, s93, 1024\n" \
  "s_min_u32 s92, %[rmax], 136\ns_mul_i32 s92, s92, %[rs]\ns_mov_b32 m0, s93\ns_add_u32 s90, %[wlo], s92\ns_addc_u32 s91, %[whi], 0\nglobal_load_lds_dwordx4 %[vs], s[90:91]\n" \
  "s_add_u32 s93, s93, 1024\ns_min_u32 s92, %[rmax], 144\ns_mul_i32 s92, s92, %[rs]\ns_mov_b32 m0, s93\ns_add_u32 s90, %[wlo], s92\ns_addc_u32 s91, %[whi], 0\n" \
  "global_load_lds_dwordx4 %[vs], s[90:91]\ns_add_u32 s93, s93, 1024\ns_min_u32 s92, %[rmax], 152\ns_mul_i32 s92, s92, %[rs]\ns_mov_b32 m0, s93\ns_add_u32 s90, %[wlo], s92\n" \
  "s_addc_u32 s91, %[whi], 0\nglobal_load_lds_dwordx4 %[vs], s[90:91]\ns_add_u32 s93, s93, 1024\ns_min_u32 s92, %[rmax], 160\ns_mul_i32 s92, s92, %[rs]\ns_mov_b32 m0, s93\n" \
  "s_add_u32 s90, %[wlo], s92\ns_addc_u32 s91, %[whi], 0\nglobal_load_lds_dwordx4 %[vs], s[90:91]\ns_add_u32 s93, s93, 1024\ns_min_u32 s92, %[rmax], 168\n" \
  "s_mul_i32 s92, s92, %[rs]\ns_mov_b32 m0, s93\ns_add_u32 s90, %[wlo], s92\ns_addc_u32 s91, %[whi], 0\nglobal_load_lds_dwordx4 %[vs], s[90:91]\ns_add_u32 s93, s93, 1024\n" \
  "s_min_u32 s92, %[rmax], 176\ns_mul_i32 s92, s92, %[rs]\ns_mov_b32 m0, s93\ns_add_u32 s90, %[wlo], s92\ns_addc_u32 s91, %[whi], 0\nglobal_load_lds_dwordx4 %[vs], s[90:91]\n" \
  "s_add_u32 s93, s93, 1024\ns_min_u32 s92, %[rmax], 184\ns_mul_i32 s92, s92, %[rs]\ns_mov_b32 m0, s93\ns_add_u32 s90, %[wlo], s92\ns_addc_u32 s91, %[whi], 0\n" \
  "global_load_lds_dwordx4 %[vs], s[90:91]\ns_add_u32 s93, s93, 1024\ns_min_u32 s92, %[rmax], 192\ns_mul_i32 s92, s92, %[rs]\ns_mov_b32 m0, s93\ns_add_u32 s90, %[wlo], s92\n" \
  "s_addc_u32 s91, %[whi], 0\nglobal_load_lds_dwordx4 %[vs], s[90:91]\ns_add_u32 s93, s93, 1024\ns_min_u32 s92, %[rmax], 200\ns_mul_i32 s92, s92, %[rs]\ns_mov_b32 m0, s93\n" \
  "s_add_u32 s90, %[wlo], s92\ns_addc_u32 s91, %[whi], 0\nglobal_load_lds_dwordx4 %[vs], s[90:91]\ns_add_u32 s93, s93, 1024\ns_min_u32 s92, %[rmax], 208\n" \
  "s_mul_i32 s92, s92, %[rs]\ns_mov_b32 m0, s93\ns_add_u32 s90, %[wlo], s92\ns_addc_u32 s91, %[whi], 0\nglobal_load_lds_dwordx4 %[vs], s[90:91]\ns_add_u32 s93, s93, 1024\n" \
  "s_min_u32 s92, %[rmax], 216\ns_mul_i32 s92, s92, %[rs]\ns_mov_b32 m0, s93\ns_add_u32 s90, %[wlo], s92\ns_addc_u32 s91, %[whi], 0\nglobal_load_lds_dwordx4 %[vs], s[90:91]\n" \
  "s_add_u32 s93, s93, 1024\ns_waitcnt vmcnt(0)\nds_read_b64_tr_b16 a[0:1], %[t0] offset:0\nds_read_b64_tr_b16 a[2:3], %[t0] offset:2048\nds_read_b64_tr_b16 a[4:5], %[t1] offset:0\n" \
  "ds_read_b64_tr_b16 a[6:7], %[t1] offset:2048\nds_read_b64_tr_b16 a[8:9], %[t2] offset:0\nds_read_b64_tr_b16 a[10:11], %[t2] offset:2048\nds_read_b64_tr_b16 a[12:13], %[t3] offset:0\n" \
  "ds_read_b64_tr_b16 a[14:15], %[t3] offset:2048\nds_read_b64_tr_b16 a[16:17], %[t0] offset:4096\nds_read_b64_tr_b16 a[18:19], %[t0] offset:6144\nds_read_b64_tr_b16 a[20:21], %[t1] offset:4096\n" \
  "ds_read_b64_tr_b16 a[22:23], %[t1] offset:6144\ns_waitcnt lgkmcnt(4)\nds_read_b64_tr_b16 a[24:25], %[t2] offset:4096\nds_read_b64_tr_b16 a[26:27], %[t2] offset:6144\n" \
  "ds_read_b64_tr_b16 a[28:29], %[t3] offset:4096\nds_read_b64_tr_b16 a[30:31], %[t3] offset:6144\nds_read_b64_tr_b16 a[32:33], %[t0] offset:8192\nds_read_b64_tr_b16 a[34:35], %[t0] offset:10240\n" \
  "ds_read_b64_tr_b16 a[36:37], %[t1] offset:8192\nds_read_b64_tr_b16 a[38:39], %[t1] offset:10240\nds_read_b64_tr_b16 a[40:41], %[t2] offset:8192\nds_read_b64_tr_b16 a[42:43], %[t2] offset:10240\n" \
  "ds_read_b64_tr_b16 a[44:45], %[t3] offset:8192\nds_read_b64_tr_b16 a[46:47], %[t3] offset:10240\ns_waitcnt lgkmcnt(4)\nds_read_b64_tr_b16 a[48:49], %[t0] offset:12288\n" \
  "ds_read_b64_tr_b16 a[50:51], %[t0] offset:14336\nds_read_b64_tr_b16 a[52:53], %[t1] offset:12288\nds_read_b64_tr_b16 a[54:55], %[t1] offset:14336\nds_read_b64_tr_b16 a[56:57], %[t2] offset:12288\n" \
  "ds_read_b64_tr_b16 a[58:59], %[t2] offset:14336\nds_read_b64_tr_b16 a[60:61], %[t3] offset:12288\nds_read_b64_tr_b16 a[62:63], %[t3] offset:14336\nds_read_b64_tr_b16 a[64:65], %[t0] offset:16384\n" \
  "ds_read_b64_tr_b16 a[66:67], %[t0] offset:18432\nds_read_b64_tr_b16 a[68:69], %[t1] offset:16384\nds_read_b64_tr_b16 a[70:71], %[t1] offset:18432\ns_waitcnt lgkmcnt(4)\n" \
  "ds_read_b64_tr_b16 a[72:73], %[t2] offset:16384\nds_read_b64_tr_b16 a[74:75], %[t2] offset:18432\nds_read_b64_tr_b16 a[76:77], %[t3] offset:16384\nds_read_b64_tr_b16 a[78:79], %[t3] offset:18432\n" \
  "ds_read_b64_tr_b16 a[80:81], %[t0] offset:20480\nds_read_b64_tr_b16 a[82:83], %[t0] offset:22528\nds_read_b64_tr_b16 a[84:85], %[t1] offset:20480\nds_read_b64_tr_b16 a[86:87], %[t1] offset:22528\n" \
  "ds_read_b64_tr_b16 a[88:89], %[t2] offset:20480\nds_read_b64_tr_b16 a[90:91], %[t2] offset:22528\nds_read_b64_tr_b16 a[92:93], %[t3] offset:20480\nds_read_b64_tr_b16 a[94:95], %[t3] offset:22528\n" \
  "s_waitcnt lgkmcnt(4)\nds_read_b64_tr_b16 a[96:97], %[t0] offset:24576\nds_read_b64_tr_b16 a[98:99], %[t0] offset:26624\nds_read_b64_tr_b16 a[100:101], %[t1] offset:24576\n" \
  "ds_read_b64_tr_b16 a[102:103], %[t1] offset:26624\nds_read_b64_tr_b16 a[104:105], %[t2] offset:24576\nds_read_b64_tr_b16 a[106:107], %[t2] offset:26624\n" \
  "ds_read_b64_tr_b16 a[108:109], %[t3] offset:24576\nds_read_b64_tr_b16 a[110:111], %[t3] offset:26624\ns_waitcnt lgkmcnt(0)\ns_mov_b32 s93, %[lw]\ns_min_u32 s92, %[rmax], 224\n" \
  "s_mul_i32 s92, s92, %[rs]\ns_mov_b32 m0, s93\ns_add_u32 s90, %[wlo], s92\ns_addc_u32 s91, %[whi], 0\nglobal_load_lds_dwordx4 %[vs], s[90:91]\ns_add_u32 s93, s93, 1024\n" \
  "s_min_u32 s92, %[rmax], 232\ns_mul_i32 s92, s92, %[rs]\ns_mov_b32 m0, s93\ns_add_u32 s90, %[wlo], s92\ns_addc_u32 s91, %[whi], 0\nglobal_load_lds_dwordx4 %[vs], s[90:91]\n" \
  "s_add_u32 s93, s93, 1024\ns_min_u32 s92, %[rmax], 240\ns_mul_i32 s92, s92, %[rs]\ns_mov_b32 m0, s93\ns_add_u32 s90, %[wlo], s92\ns_addc_u32 s91, %[whi], 0\n" \
  "global_load_lds_dwordx4 %[vs], s[90:91]\ns_add_u32 s93, s93, 1024\ns_min_u32 s92, %[rmax], 248\ns_mul_i32 s92, s92, %[rs]\ns_mov_b32 m0, s93\ns_add_u32 s90, %[wlo], s92\n" \
  "s_addc_u32 s91, %[whi], 0\nglobal_load_lds_dwordx4 %[vs], s[90:91]\ns_add_u32 s93, s93, 1024\ns_min_u32 s92, %[rmax], 256\ns_mul_i32 s92, s92, %[rs]\ns_mov_b32 m0, s93\n" \
  "s_add_u32 s90, %[wlo], s92\ns_addc_u32 s91, %[whi], 0\nglobal_load_lds_dwordx4 %[vs], s[90:91]\ns_add_u32 s93, s93, 1024\ns_min_u32 s92, %[rmax], 264\n" \
  "s_mul_i32 s92, s92, %[rs]\ns_mov_b32 m0, s93\ns_add_u32 s90, %[wlo], s92\ns_addc_u32 s91, %[whi], 0\nglobal_load_lds_dwordx4 %[vs], s[90:91]\ns_add_u32 s93, s93, 1024\n" \
  "s_min_u32 s92, %[rmax], 272\ns_mul_i32 s92, s92, %[rs]\ns_mov_b32 m0, s93\ns_add_u32 s90, %[wlo], s92\ns_addc_u32 s91, %[whi], 0\nglobal_load_lds_dwordx4 %[vs], s[90:91]\n" \
  "s_add_u32 s93, s93, 1024\ns_min_u32 s92, %[rmax], 280\ns_mul_i32 s92, s92, %[rs]\ns_mov_b32 m0, s93\ns_add_u32 s90, %[wlo], s92\ns_addc_u32 s91, %[whi], 0\n" \
  "global_load_lds_dwordx4 %[vs], s[90:91]\ns_add_u32 s93, s93, 1024\ns_min_u32 s92, %[rmax], 288\ns_mul_i32 s92, s92, %[rs]\ns_mov_b32 m0, s93\ns_add_u32 s90, %[wlo], s92\n" \
  "s_addc_u32 s91, %[whi], 0\nglobal_load_lds_dwordx4 %[vs], s[90:91]\ns_add_u32 s93, s93, 1024\ns_min_u32 s92, %[rmax], 296\ns_mul_i32 s92, s92, %[rs]\ns_mov_b32 m0, s93\n" \
  "s_add_u32 s90, %[wlo], s92\ns_addc_u32 s91, %[whi], 0\nglobal_load_lds_dwordx4 %[vs], s[90:91]\ns_add_u32 s93, s93, 1024\ns_min_u32 s92, %[rmax], 304\n" \
  "s_mul_i32 s92, s92, %[rs]\ns_mov_b32 m0, s93\ns_add_u32 s90, %[wlo], s92\ns_addc_u32 s91, %[whi], 0\nglobal_load_lds_dwordx4 %[vs], s[90:91]\ns_add_u32 s93, s93, 1024\n" \
  "s_min_u32 s92, %[rmax], 312\ns_mul_i32 s92, s92, %[rs]\ns_mov_b32 m0, s93\ns_add_u32 s90, %[wlo], s92\ns_addc_u32 s91, %[whi], 0\nglobal_load_lds_dwordx4 %[vs], s[90:91]\n" \
  "s_add_u32 s93, s93, 1024\ns_min_u32 s92, %[rmax], 320\ns_mul_i32 s92, s92, %[rs]\ns_mov_b32 m0, s93\ns_add_u32 s90, %[wlo], s92\ns_addc_u32 s91, %[whi], 0\n" \
  "global_load_lds_dwordx4 %[vs], s[90:91]\ns_add_u32 s93, s93, 1024\ns_min_u32 s92, %[rmax], 328\ns_mul_i32 s92, s92, %[rs]\ns_mov_b32 m0, s93\ns_add_u32 s90, %[wlo], s92\n" \
  "s_addc_u32 s91, %[whi], 0\nglobal_load_lds_dwordx4 %[vs], s[90:91]\ns_add_u32 s93, s93, 1024\ns_min_u32 s92, %[rmax], 336\ns_mul_i32 s92, s92, %[rs]\ns_mov_b32 m0, s93\n" \
  "s_add_u32 s90, %[wlo], s92\ns_addc_u32 s91, %[whi], 0\nglobal_load_lds_dwordx4 %[vs], s[90:91]\ns_add_u32 s93, s93, 1024\ns_min_u32 s92, %[rmax], 344\n" \
  "s_mul_i32 s92, s92, %[rs]\ns_mov_b32 m0, s93\ns_add_u32 s90, %[wlo], s92\ns_addc_u32 s91, %[whi], 0\nglobal_load_lds_dwordx4 %[vs], s[90:91]\ns_add_u32 s93, s93, 1024\n" \
  "s_min_u32 s92, %[rmax], 352\ns_mul_i32 s92, s92, %[rs]\ns_mov_b32 m0, s93\ns_add_u32 s90, %[wlo], s92\ns_addc_u32 s91, %[whi], 0\nglobal_load_lds_dwordx4 %[vs], s[90:91]\n" \
  "s_add_u32 s93, s93, 1024\ns_min_u32 s92, %[rmax], 360\ns_mul_i32 s92, s92, %[rs]\ns_mov_b32 m0, s93\ns_add_u32 s90, %[wlo], s92\ns_addc_u32 s91, %[whi], 0\n" \
  "global_load_lds_dwordx4 %[vs], s[90:91]\ns_add_u32 s93, s93, 1024\ns_min_u32 s92, %[rmax], 368\ns_mul_i32 s92, s92, %[rs]\ns_mov_b32 m0, s93\ns_add_u32 s90, %[wlo], s92\n" \
  "s_addc_u32 s91, %[whi], 0\nglobal_load_lds_dwordx4 %[vs], s[90:91]\ns_add_u32 s93, s93, 1024\ns_min_u32 s92, %[rmax], 376\ns_mul_i32 s92, s92, %[rs]\ns_mov_b32 m0, s93\n" \
  "s_add_u32 s90, %[wlo], s92\ns_addc_u32 s91, %[whi], 0\nglobal_load_lds_dwordx4 %[vs], s[90:91]\ns_add_u32 s93, s93, 1024\ns_min_u32 s92, %[rmax], 384\n" \
  "s_mul_i32 s92, s92, %[rs]\ns_mov_b32 m0, s93\ns_add_u32 s90, %[wlo], s92\ns_addc_u32 s91, %[whi], 0\nglobal_load_lds_dwordx4 %[vs], s[90:91]\ns_add_u32 s93, s93, 1024\n" \
  "s_min_u32 s92, %[rmax], 392\ns_mul_i32 s92, s92, %[rs]\ns_mov_b32 m0, s93\ns_add_u32 s90, %[wlo], s92\ns_addc_u32 s91, %[whi], 0\nglobal_load_lds_dwordx4 %[vs], s[90:91]\n" \
  "s_add_u32 s93, s93, 1024\ns_min_u32 s92, %[rmax], 400\ns_mul_i32 s92, s92, %[rs]\ns_mov_b32 m0, s93\ns_add_u32 s90, %[wlo], s92\ns_addc_u32 s91, %[whi], 0\n" \
  "global_load_lds_dwordx4 %[vs], s[90:91]\ns_add_u32 s93, s93, 1024\ns_min_u32 s92, %[rmax], 408\ns_mul_i32 s92, s92, %[rs]\ns_mov_b32 m0, s93\ns_add_u32 s90, %[wlo], s92\n" \
  "s_addc_u32 s91, %[whi], 0\nglobal_load_lds_dwordx4 %[vs], s[90:91]\ns_add_u32 s93, s93, 1024\ns_min_u32 s92, %[rmax], 416\ns_mul_i32 s92, s92, %[rs]\ns_mov_b32 m0, s93\n" \
  "s_add_u32 s90, %[wlo], s92\ns_addc_u32 s91, %[whi], 0\nglobal_load_lds_dwordx4 %[vs], s[90:91]\ns_add_u32 s93, s93, 1024\ns_min_u32 s92, %[rmax], 424\n" \
  "s_mul_i32 s92, s92, %[rs]\ns_mov_b32 m0, s93\ns_add_u32 s90, %[wlo], s92\ns_addc_u32 s91, %[whi], 0\nglobal_load_lds_dwordx4 %[vs], s[90:91]\ns_add_u32 s93, s93, 1024\n" \
  "s_min_u32 s92, %[rmax], 432\ns_mul_i32 s92, s92, %[rs]\ns_mov_b32 m0, s93\ns_add_u32 s90, %[wlo], s92\ns_addc_u32 s91, %[whi], 0\nglobal_load_lds_dwordx4 %[vs], s[90:91]\n" \
  "s_add_u32 s93, s93, 1024\ns_min_u32 s92, %[rmax], 440\ns_mul_i32 s92, s92, %[rs]\ns_mov_b32 m0, s93\ns_add_u32 s90, %[wlo], s92\ns_addc_u32 s91, %[whi], 0\n" \
  "global_load_lds_dwordx4 %[vs], s[90:91]\ns_add_u32 s93, s93, 1024\ns_waitcnt vmcnt(0)\nds_read_b64_tr_b16 a[112:113], %[t0] offset:0\nds_read_b64_tr_b16 a[114:115], %[t0] offset:2048\n" \
  "ds_read_b64_tr_b16 a[116:117], %[t1] offset:0\nds_read_b64_tr_b16 a[118:119], %[t1] offset:2048\nds_read_b64_tr_b16 a[120:121], %[t2] offset:0\nds_read_b64_tr_b16 a[122:123], %[t2] offset:2048\n" \
  "ds_read_b64_tr_b16 a[124:125], %[t3] offset:0\nds_read_b64_tr_b16 a[126:127], %[t3] offset:2048\nds_read_b64_tr_b16 a[128:129], %[t0] offset:4096\nds_read_b64_tr_b16 a[130:131], %[t0] offset:6144\n" \
  "ds_read_b64_tr_b16 a[132:133], %[t1] offset:4096\nds_read_b64_tr_b16 a[134:135], %[t1] offset:6144\ns_waitcnt lgkmcnt(4)\nds_read_b64_tr_b16 a[136:137], %[t2] offset:4096\n" \
  "ds_read_b64_tr_b16 a[138:139], %[t2] offset:6144\nds_read_b64_tr_b16 a[140:141], %[t3] offset:4096\nds_read_b64_tr_b16 a[142:143], %[t3] offset:6144\nds_read_b64_tr_b16 a[144:145], %[t0] offset:8192\n" \
  "ds_read_b64_tr_b16 a[146:147], %[t0] offset:10240\nds_read_b64_tr_b16 a[148:149], %[t1] offset:8192\nds_read_b64_tr_b16 a[150:151], %[t1] offset:10240\n" \
  "ds_read_b64_tr_b16 a[152:153], %[t2] offset:8192\nds_read_b64_tr_b16 a[154:155], %[t2] offset:10240\nds_read_b64_tr_b16 a[156:157], %[t3] offset:8192\n" \
  "ds_read_b64_tr_b16 a[158:159], %[t3] offset:10240\ns_waitcnt lgkmcnt(4)\nds_read_b64_tr_b16 a[160:161], %[t0] offset:12288\nds_read_b64_tr_b16 a[162:163], %[t0] offset:14336\n" \
  "ds_read_b64_tr_b16 a[164:165], %[t1] offset:12288\nds_read_b64_tr_b16 a[166:167], %[t1] offset:14336\nds_read_b64_tr_b16 a[168:169], %[t2] offset:12288\n" \
  "ds_read_b64_tr_b16 a[170:171], %[t2] offset:14336\nds_read_b64_tr_b16 a[172:173], %[t3] offset:12288\nds_read_b64_tr_b16 a[174:175], %[t3] offset:14336\n" \
  "ds_read_b64_tr_b16 a[176:177], %[t0] offset:16384\nds_read_b64_tr_b16 a[178:179], %[t0] offset:18432\nds_read_b64_tr_b16 a[180:181], %[t1] offset:16384\n" \
  "ds_read_b64_tr_b16 a[182:183], %[t1] offset:18432\ns_waitcnt lgkmcnt(4)\nds_read_b64_tr_b16 a[184:185], %[t2] offset:16384\nds_read_b64_tr_b16 a[186:187], %[t2] offset:18432\n" \
  "ds_read_b64_tr_b16 a[188:189], %[t3] offset:16384\nds_read_b64_tr_b16 a[190:191], %[t3] offset:18432\nds_read_b64_tr_b16 a[192:193], %[t0] offset:20480\n" \
  "ds_read_b64_tr_b16 a[194:195], %[t0] offset:22528\nds_read_b64_tr_b16 a[196:197], %[t1] offset:20480\nds_read_b64_tr_b16 a[198:199], %[t1] offset:22528\n" \
  "ds_read_b64_tr_b16 a[200:201], %[t2] offset:20480\nds_read_b64_tr_b16 a[202:203], %[t2] offset:22528\nds_read_b64_tr_b16 a[204:205], %[t3] offset:20480\n" \
  "ds_read_b64_tr_b16 a[206:207], %[t3] offset:22528\ns_waitcnt lgkmcnt(4)\nds_read_b64_tr_b16 a[208:209], %[t0] offset:24576\nds_read_b64_tr_b16 a[210:211], %[t0] offset:26624\n" \
  "ds_read_b64_tr_b16 a[212:213], %[t1] offset:24576\nds_read_b64_tr_b16 a[214:215], %[t1] offset:26624\nds_read_b64_tr_b16 a[216:217], %[t2] offset:24576\n" \
  "ds_read_b64_tr_b16 a[218:219], %[t2] offset:26624\nds_read_b64_tr_b16 a[220:221], %[t3] offset:24576\nds_read_b64_tr_b16 a[222:223], %[t3] offset:26624\n" \
  "s_waitcnt lgkmcnt(0)\n"
#define R2_PRE_ASM \
  "s_mov_b64 s[90:91], %[src]\nglobal_load_dwordx4 v[128:131], %[l16], s[90:91] offset:-4096 sc1\nglobal_load_dwordx4 v[132:135], %[l16], s[90:91] offset:-3072 sc1\n" \
  "global_load_dwordx4 v[136:139], %[l16], s[90:91] offset:-2048 sc1\nglobal_load_dwordx4 v[140:143], %[l16], s[90:91] offset:-1024 sc1\nglobal_load_dwordx4 v[144:147], %[l16], s[90:91] offset:0 sc1\n" \
  "global_load_dwordx4 v[148:151], %[l16], s[90:91] offset:1024 sc1\nglobal_load_dwordx4 v[152:155], %[l16], s[90:91] offset:2048 sc1\nglobal_load_dwordx4 v[156:159], %[l16], s[90:91] offset:3072 sc1\n" \
  "s_add_u32 s90, s90, 16384\ns_addc_u32 s91, s91, 0\nglobal_load_dwordx4 v[160:163], %[l16], s[90:91] offset:-4096 sc1\nglobal_load_dwordx4 v[164:167], %[l16], s[90:91] offset:-3072 sc1\n" \
  "global_load_dwordx4 v[168:171], %[l16], s[90:91] offset:-2048 sc1\nglobal_load_dwordx4 v[172:175], %[l16], s[90:91] offset:-1024 sc1\nglobal_load_dwordx4 v[176:179], %[l16], s[90:91] offset:0 sc1\n" \
  "global_load_dwordx4 v[180:183], %[l16], s[90:91] offset:1024 sc1\nglobal_load_dwordx4 v[184:187], %[l16], s[90:91] offset:2048 sc1\nglobal_load_dwordx4 v[188:191], %[l16], s[90:91] offset:3072 sc1\n" \
  "s_add_u32 s90, s90, 16384\ns_addc_u32 s91, s91, 0\nglobal_load_dwordx4 v[192:195], %[l16], s[90:91] offset:-4096 sc1\nglobal_load_dwordx4 v[196:199], %[l16], s[90:91] offset:-3072 sc1\n" \
  "global_load_dwordx4 v[200:203], %[l16], s[90:91] offset:-2048 sc1\nglobal_load_dwordx4 v[204:207], %[l16], s[90:91] offset:-1024 sc1\nglobal_load_dwordx4 v[208:211], %[l16], s[90:91] offset:0 sc1\n" \
  "global_load_dwordx4 v[212:215], %[l16], s[90:91] offset:1024 sc1\nglobal_load_dwordx4 v[216:219], %[l16], s[90:91] offset:2048 sc1\nglobal_load_dwordx4 v[220:223], %[l16], s[90:91] offset:3072 sc1\n" \
  "s_add_u32 s90, s90, 16384\ns_addc_u32 s91, s91, 0\nglobal_load_dwordx4 v[224:227], %[l16], s[90:91] offset:-4096 sc1\nglobal_load_dwordx4 v[228:231], %[l16], s[90:91] offset:-3072 sc1\n" \
  "global_load_dwordx4 v[232:235], %[l16], s[90:91] offset:-2048 sc1\nglobal_load_dwordx4 v[236:239], %[l16], s[90:91] offset:-1024 sc1\n"
#define R2_RUN_ASM \
  "s_waitcnt vmcnt(27)\nv_mfma_f32_16x16x32_bf16 %[c0], a[0:3], v[128:131], 0\nv_mfma_f32_16x16x32_bf16 %[c1], a[4:7], v[128:131], 0\nv_mfma_f32_16x16x32_bf16 %[c2], a[8:11], v[128:131], 0\n" \
  "v_mfma_f32_16x16x32_bf16 %[c3], a[12:15], v[128:131], 0\ns_mov_b64 s[90:91], %[src2]\nglobal_load_dwordx4 v[128:131], %[l16], s[90:91] offset:-4096 sc1\n" \
  "s_waitcnt vmcnt(27)\nv_mfma_f32_16x16x32_bf16 %[c4], a[0:3], v[132:135], 0\nv_mfma_f32_16x16x32_bf16 %[c5], a[4:7], v[132:135], 0\nv_mfma_f32_16x16x32_bf16 %[c6], a[8:11], v[132:135], 0\n" \
  "v_mfma_f32_16x16x32_bf16 %[c7], a[12:15], v[132:135], 0\nglobal_load_dwordx4 v[132:135], %[l16], s[90:91] offset:-3072 sc1\ns_waitcnt vmcnt(27)\nv_mfma_f32_16x16x32_bf16 %[c0], a[16:19], v[136:139], %[c0]\n" \
  "v_mfma_f32_16x16x32_bf16 %[c1], a[20:23], v[136:139], %[c1]\nv_mfma_f32_16x16x32_bf16 %[c2], a[24:27], v[136:139], %[c2]\nv_mfma_f32_16x16x32_bf16 %[c3], a[28:31], v[136:139], %[c3]\n" \
  "global_load_dwordx4 v[136:139], %[l16], s[90:91] offset:-2048 sc1\ns_waitcnt vmcnt(27)\nv_mfma_f32_16x16x32_bf16 %[c4], a[16:19], v[140:143], %[c4]\nv_mfma_f32_16x16x32_bf16 %[c5], a[20:23], v[140:143], %[c5]\n" \
  "v_mfma_f32_16x16x32_bf16 %[c6], a[24:27], v[140:143], %[c6]\nv_mfma_f32_16x16x32_bf16 %[c7], a[28:31], v[140:143], %[c7]\nglobal_load_dwordx4 v[140:143], %[l16], s[90:91] offset:-1024 sc1\n" \
  "s_waitcnt vmcnt(27)\nv_mfma_f32_16x16x32_bf16 %[c0], a[32:35], v[144:147], %[c0]\nv_mfma_f32_16x16x32_bf16 %[c1], a[36:39], v[144:147], %[c1]\nv_mfma_f32_16x16x32_bf16 %[c2], a[40:43], v[144:147], %[c2]\n" \
  "v_mfma_f32_16x16x32_bf16 %[c3], a[44:47], v[144:147], %[c3]\nglobal_load_dwordx4 v[144:147], %[l16], s[90:91] offset:0 sc1\ns_waitcnt vmcnt(27)\nv_mfma_f32_16x16x32_bf16 %[c4], a[32:35], v[148:151], %[c4]\n" \
  "v_mfma_f32_16x16x32_bf16 %[c5], a[36:39], v[148:151], %[c5]\nv_mfma_f32_16x16x32_bf16 %[c6], a[40:43], v[148:151], %[c6]\nv_mfma_f32_16x16x32_bf16 %[c7], a[44:47], v[148:151], %[c7]\n" \
  "global_load_dwordx4 v[148:151], %[l16], s[90:91] offset:1024 sc1\ns_waitcnt vmcnt(27)\nv_mfma_f32_16x16x32_bf16 %[c0], a[48:51], v[152:155], %[c0]\nv_mfma_f32_16x16x32_bf16 %[c1], a[52:55], v[152:155], %[c1]\n" \
  "v_mfma_f32_16x16x32_bf16 %[c2], a[56:59], v[152:155], %[c2]\nv_mfma_f32_16x16x32_bf16 %[c3], a[60:63], v[152:155], %[c3]\nglobal_load_dwordx4 v[152:155], %[l16], s[90:91] offset:2048 sc1\n" \
  "s_waitcnt vmcnt(27)\nv_mfma_f32_16x16x32_bf16 %[c4], a[48:51], v[156:159], %[c4]\nv_mfma_f32_16x16x32_bf16 %[c5], a[52:55], v[156:159], %[c5]\nv_mfma_f32_16x16x32_bf16 %[c6], a[56:59], v[156:159], %[c6]\n" \
  "v_mfma_f32_16x16x32_bf16 %[c7], a[60:63], v[156:159], %[c7]\nglobal_load_dwordx4 v[156:159], %[l16], s[90:91] offset:3072 sc1\ns_waitcnt vmcnt(27)\nv_mfma_f32_16x16x32_bf16 %[c0], a[64:67], v[160:163], %[c0]\n" \
  "v_mfma_f32_16x16x32_bf16 %[c1], a[68:71], v[160:163], %[c1]\nv_mfma_f32_16x16x32_bf16 %[c2], a[72:75], v[160:163], %[c2]\nv_mfma_f32_16x16x32_bf16 %[c3], a[76:79], v[160:163], %[c3]\n" \
  "s_add_u32 s90, s90, 16384\ns_addc_u32 s91, s91, 0\nglobal_load_dwordx4 v[160:163], %[l16], s[90:91] offset:-4096 sc1\ns_waitcnt vmcnt(27)\nv_mfma_f32_16x16x32_bf16 %[c4], a[64:67], v[164:167], %[c4]\n" \
  "v_mfma_f32_16x16x32_bf16 %[c5], a[68:71], v[164:167], %[c5]\nv_mfma_f32_16x16x32_bf16 %[c6], a[72:75], v[164:167], %[c6]\nv_mfma_f32_16x16x32_bf16 %[c7], a[76:79], v[164:167], %[c7]\n" \
  "global_load_dwordx4 v[164:167], %[l16], s[90:91] offset:-3072 sc1\ns_waitcnt vmcnt(27)\nv_mfma_f32_16x16x32_bf16 %[c0], a[80:83], v[168:171], %[c0]\nv_mfma_f32_16x16x32_bf16 %[c1], a[84:87], v[168:171], %[c1]\n" \
  "v_mfma_f32_16x16x32_bf16 %[c2], a[88:91], v[168:171], %[c2]\nv_mfma_f32_16x16x32_bf16 %[c3], a[92:95], v[168:171], %[c3]\nglobal_load_dwordx4 v[168:171], %[l16], s[90:91] offset:-2048 sc1\n" \
  "s_waitcnt vmcnt(27)\nv_mfma_f32_16x16x32_bf16 %[c4], a[80:83], v[172:175], %[c4]\nv_mfma_f32_16x16x32_bf16 %[c5], a[84:87], v[172:175], %[c5]\nv_mfma_f32_16x16x32_bf16 %[c6], a[88:91], v[172:175], %[c6]\n" \
  "v_mfma_f32_16x16x32_bf16 %[c7], a[92:95], v[172:175], %[c7]\nglobal_load_dwordx4 v[172:175], %[l16], s[90:91] offset:-1024 sc1\ns_waitcnt vmcnt(27)\nv_mfma_f32_16x16x32_bf16 %[c0], a[96:99], v[176:179], %[c0]\n" \
  "v_mfma_f32_16x16x32_bf16 %[c1], a[100:103], v[176:179], %[c1]\nv_mfma_f32_16x16x32_bf16 %[c2], a[104:107], v[176:179], %[c2]\nv_mfma_f32_16x16x32_bf16 %[c3], a[108:111], v[176:179], %[c3]\n" \
  "global_load_dwordx4 v[176:179], %[l16], s[90:91] offset:0 sc1\ns_waitcnt vmcnt(27)\nv_mfma_f32_16x16x32_bf16 %[c4], a[96:99], v[180:183], %[c4]\nv_mfma_f32_16x16x32_bf16 %[c5], a[100:103], v[180:183], %[c5]\n" \
  "v_mfma_f32_16x16x32_bf16 %[c6], a[104:107], v[180:183], %[c6]\nv_mfma_f32_16x16x32_bf16 %[c7], a[108:111], v[180:183], %[c7]\nglobal_load_dwordx4 v[180:183], %[l16], s[90:91] offset:1024 sc1\n" \
  "s_waitcnt vmcnt(27)\nv_mfma_f32_16x16x32_bf16 %[c0], a[112:115], v[184:187], %[c0]\nv_mfma_f32_16x16x32_bf16 %[c1], a[116:119], v[184:187], %[c1]\nv_mfma_f32_16x16x32_bf16 %[c2], a[120:123], v[184:187], %[c2]\n" \
  "v_mfma_f32_16x16x32_bf16 %[c3], a[124:127], v[184:187], %[c3]\nglobal_load_dwordx4 v[184:187], %[l16], s[90:91] offset:2048 sc1\ns_waitcnt vmcnt(27)\nv_mfma_f32_16x16x32_bf16 %[c4], a[112:115], v[188:191], %[c4]\n" \
  "v_mfma_f32_16x16x32_bf16 %[c5], a[116:119], v[188:191], %[c5]\nv_mfma_f32_16x16x32_bf16 %[c6], a[120:123], v[188:191], %[c6]\nv_mfma_f32_16x16x32_bf16 %[c7], a[124:127], v[188:191], %[c7]\n" \
  "global_load_dwordx4 v[188:191], %[l16], s[90:91] offset:3072 sc1\ns_waitcnt vmcnt(27)\nv_mfma_f32_16x16x32_bf16 %[c0], a[128:131], v[192:195], %[c0]\nv_mfma_f32_16x16x32_bf16 %[c1], a[132:135], v[192:195], %[c1]\n" \
  "v_mfma_f32_16x16x32_bf16 %[c2], a[136:139], v[192:195], %[c2]\nv_mfma_f32_16x16x32_bf16 %[c3], a[140:143], v[192:195], %[c3]\ns_add_u32 s90, s90, 16384\n" \
  "s_addc_u32 s91, s91, 0\nglobal_load_dwordx4 v[192:195], %[l16], s[90:91] offset:-4096 sc1\ns_waitcnt vmcnt(27)\nv_mfma_f32_16x16x32_bf16 %[c4], a[128:131], v[196:199], %[c4]\n" \
  "v_mfma_f32_16x16x32_bf16 %[c5], a[132:135], v[196:199], %[c5]\nv_mfma_f32_16x16x32_bf16 %[c6], a[136:139], v[196:199], %[c6]\nv_mfma_f32_16x16x32_bf16 %[c7], a[140:143], v[196:199], %[c7]\n" \
  "global_load_dwordx4 v[196:199], %[l16], s[90:91] offset:-3072 sc1\ns_waitcnt vmcnt(27)\nv_mfma_f32_16x16x32_bf16 %[c0], a[144:147], v[200:203], %[c0]\n" \
  "v_mfma_f32_16x16x32_bf16 %[c1], a[148:151], v[200:203], %[c1]\nv_mfma_f32_16x16x32_bf16 %[c2], a[152:155], v[200:203], %[c2]\nv_mfma_f32_16x16x32_bf16 %[c3], a[156:159], v[200:203], %[c3]\n" \
  "global_load_dwordx4 v[200:203], %[l16], s[90:91] offset:-2048 sc1\ns_waitcnt vmcnt(27)\nv_mfma_f32_16x16x32_bf16 %[c4], a[144:147], v[204:207], %[c4]\n" \
  "v_mfma_f32_16x16x32_bf16 %[c5], a[148:151], v[204:207], %[c5]\nv_mfma_f32_16x16x32_bf16 %[c6], a[152:155], v[204:207], %[c6]\nv_mfma_f32_16x16x32_bf16 %[c7], a[156:159], v[204:207], %[c7]\n" \
  "global_load_dwordx4 v[204:207], %[l16], s[90:91] offset:-1024 sc1\ns_waitcnt vmcnt(27)\nv_mfma_f32_16x16x32_bf16 %[c0], a[160:163], v[208:211], %[c0]\n" \
  "v_mfma_f32_16x16x32_bf16 %[c1], a[164:167], v[208:211], %[c1]\nv_mfma_f32_16x16x32_bf16 %[c2], a[168:171], v[208:211], %[c2]\nv_mfma_f32_16x16x32_bf16 %[c3], a[172:175], v[208:211], %[c3]\n" \
  "global_load_dwordx4 v[208:211], %[l16], s[90:91] offset:0 sc1\ns_waitcnt vmcnt(27)\nv_mfma_f32_16x16x32_bf16 %[c4], a[160:163], v[212:215], %[c4]\nv_mfma_f32_16x16x32_bf16 %[c5], a[164:167], v[212:215], %[c5]\n" \
  "v_mfma_f32_16x16x32_bf16 %[c6], a[168:171], v[212:215], %[c6]\nv_mfma_f32_16x16x32_bf16 %[c7], a[172:175], v[212:215], %[c7]\nglobal_load_dwordx4 v[212:215], %[l16], s[90:91] offset:1024 sc1\n" \
  "s_waitcnt vmcnt(27)\nv_mfma_f32_16x16x32_bf16 %[c0], a[176:179], v[216:219], %[c0]\nv_mfma_f32_16x16x32_bf16 %[c1], a[180:183], v[216:219], %[c1]\nv_mfma_f32_16x16x32_bf16 %[c2], a[184:187], v[216:219], %[c2]\n" \
  "v_mfma_f32_16x16x32_bf16 %[c3], a[188:191], v[216:219], %[c3]\nglobal_load_dwordx4 v[216:219], %[l16], s[90:91] offset:2048 sc1\ns_waitcnt vmcnt(27)\nv_mfma_f32_16x16x32_bf16 %[c4], a[176:179], v[220:223], %[c4]\n" \
  "v_mfma_f32_16x16x32_bf16 %[c5], a[180:183], v[220:223], %[c5]\nv_mfma_f32_16x16x32_bf16 %[c6], a[184:187], v[220:223], %[c6]\nv_mfma_f32_16x16x32_bf16 %[c7], a[188:191], v[220:223], %[c7]\n" \
  "global_load_dwordx4 v[220:223], %[l16], s[90:91] offset:3072 sc1\ns_waitcnt vmcnt(27)\nv_mfma_f32_16x16x32_bf16 %[c0], a[192:195], v[224:227], %[c0]\nv_mfma_f32_16x16x32_bf16 %[c1], a[196:199], v[224:227], %[c1]\n" \
  "v_mfma_f32_16x16x32_bf16 %[c2], a[200:203], v[224:227], %[c2]\nv_mfma_f32_16x16x32_bf16 %[c3], a[204:207], v[224:227], %[c3]\ns_add_u32 s90, s90, 16384\n" \
  "s_addc_u32 s91, s91, 0\nglobal_load_dwordx4 v[224:227], %[l16], s[90:91] offset:-4096 sc1\ns_waitcnt vmcnt(27)\nv_mfma_f32_16x16x32_bf16 %[c4], a[192:195], v[228:231], %[c4]\n" \
  "v_mfma_f32_16x16x32_bf16 %[c5], a[196:199], v[228:231], %[c5]\nv_mfma_f32_16x16x32_bf16 %[c6], a[200:203], v[228:231], %[c6]\nv_mfma_f32_16x16x32_bf16 %[c7], a[204:207], v[228:231], %[c7]\n" \
  "global_load_dwordx4 v[228:231], %[l16], s[90:91] offset:-3072 sc1\ns_waitcnt vmcnt(27)\nv_mfma_f32_16x16x32_bf16 %[c0], a[208:211], v[232:235], %[c0]\n" \
  "v_mfma_f32_16x16x32_bf16 %[c1], a[212:215], v[232:235], %[c1]\nv_mfma_f32_16x16x32_bf16 %[c2], a[216:219], v[232:235], %[c2]\nv_mfma_f32_16x16x32_bf16 %[c3], a[220:223], v[232:235], %[c3]\n" \
  "global_load_dwordx4 v[232:235], %[l16], s[90:91] offset:-2048 sc1\ns_waitcnt vmcnt(27)\nv_mfma_f32_16x16x32_bf16 %[c4], a[208:211], v[236:239], %[c4]\n" \
  "v_mfma_f32_16x16x32_bf16 %[c5], a[212:215], v[236:239], %[c5]\nv_mfma_f32_16x16x32_bf16 %[c6], a[216:219], v[236:239], %[c6]\nv_mfma_f32_16x16x32_bf16 %[c7], a[220:223], v[236:239], %[c7]\n" \
  "global_load_dwordx4 v[236:239], %[l16], s[90:91] offset:-1024 sc1\ns_waitcnt vmcnt(27)\nv_mfma_f32_16x16x32_bf16 %[c8], a[0:3], v[128:131], 0\nv_mfma_f32_16x16x32_bf16 %[c9], a[4:7], v[128:131], 0\n" \
  "v_mfma_f32_16x16x32_bf16 %[c10], a[8:11], v[128:131], 0\nv_mfma_f32_16x16x32_bf16 %[c11], a[12:15], v[128:131], 0\nv_mov_b32 v240, 1\ns_mov_b64 exec, 1\n" \
  "global_store_dword %[l16], v240, %[fl] sc1\ns_mov_b64 exec, -1\ns_waitcnt vmcnt(27)\nv_mfma_f32_16x16x32_bf16 %[c12], a[0:3], v[132:135], 0\nv_mfma_f32_16x16x32_bf16 %[c13], a[4:7], v[132:135], 0\n" \
  "v_mfma_f32_16x16x32_bf16 %[c14], a[8:11], v[132:135], 0\nv_mfma_f32_16x16x32_bf16 %[c15], a[12:15], v[132:135], 0\ns_waitcnt vmcnt(26)\nv_mfma_f32_16x16x32_bf16 %[c8], a[16:19], v[136:139], %[c8]\n" \
  "v_mfma_f32_16x16x32_bf16 %[c9], a[20:23], v[136:139], %[c9]\nv_mfma_f32_16x16x32_bf16 %[c10], a[24:27], v[136:139], %[c10]\nv_mfma_f32_16x16x32_bf16 %[c11], a[28:31], v[136:139], %[c11]\n" \
  "s_waitcnt vmcnt(25)\nv_mfma_f32_16x16x32_bf16 %[c12], a[16:19], v[140:143], %[c12]\nv_mfma_f32_16x16x32_bf16 %[c13], a[20:23], v[140:143], %[c13]\nv_mfma_f32_16x16x32_bf16 %[c14], a[24:27], v[140:143], %[c14]\n" \
  "v_mfma_f32_16x16x32_bf16 %[c15], a[28:31], v[140:143], %[c15]\ns_waitcnt vmcnt(24)\nv_mfma_f32_16x16x32_bf16 %[c8], a[32:35], v[144:147], %[c8]\nv_mfma_f32_16x16x32_bf16 %[c9], a[36:39], v[144:147], %[c9]\n" \
  "v_mfma_f32_16x16x32_bf16 %[c10], a[40:43], v[144:147], %[c10]\nv_mfma_f32_16x16x32_bf16 %[c11], a[44:47], v[144:147], %[c11]\ns_waitcnt vmcnt(23)\nv_mfma_f32_16x16x32_bf16 %[c12], a[32:35], v[148:151], %[c12]\n" \
  "v_mfma_f32_16x16x32_bf16 %[c13], a[36:39], v[148:151], %[c13]\nv_mfma_f32_16x16x32_bf16 %[c14], a[40:43], v[148:151], %[c14]\nv_mfma_f32_16x16x32_bf16 %[c15], a[44:47], v[148:151], %[c15]\n" \
  "s_waitcnt vmcnt(22)\nv_mfma_f32_16x16x32_bf16 %[c8], a[48:51], v[152:155], %[c8]\nv_mfma_f32_16x16x32_bf16 %[c9], a[52:55], v[152:155], %[c9]\nv_mfma_f32_16x16x32_bf16 %[c10], a[56:59], v[152:155], %[c10]\n" \
  "v_mfma_f32_16x16x32_bf16 %[c11], a[60:63], v[152:155], %[c11]\ns_waitcnt vmcnt(21)\nv_mfma_f32_16x16x32_bf16 %[c12], a[48:51], v[156:159], %[c12]\nv_mfma_f32_16x16x32_bf16 %[c13], a[52:55], v[156:159], %[c13]\n" \
  "v_mfma_f32_16x16x32_bf16 %[c14], a[56:59], v[156:159], %[c14]\nv_mfma_f32_16x16x32_bf16 %[c15], a[60:63], v[156:159], %[c15]\ns_waitcnt vmcnt(20)\nv_mfma_f32_16x16x32_bf16 %[c8], a[64:67], v[160:163], %[c8]\n" \
  "v_mfma_f32_16x16x32_bf16 %[c9], a[68:71], v[160:163], %[c9]\nv_mfma_f32_16x16x32_bf16 %[c10], a[72:75], v[160:163], %[c10]\nv_mfma_f32_16x16x32_bf16 %[c11], a[76:79], v[160:163], %[c11]\n" \
  "s_waitcnt vmcnt(19)\nv_mfma_f32_16x16x32_bf16 %[c12], a[64:67], v[164:167], %[c12]\nv_mfma_f32_16x16x32_bf16 %[c13], a[68:71], v[164:167], %[c13]\nv_mfma_f32_16x16x32_bf16 %[c14], a[72:75], v[164:167], %[c14]\n" \
  "v_mfma_f32_16x16x32_bf16 %[c15], a[76:79], v[164:167], %[c15]\ns_waitcnt vmcnt(18)\nv_mfma_f32_16x16x32_bf16 %[c8], a[80:83], v[168:171], %[c8]\nv_mfma_f32_16x16x32_bf16 %[c9], a[84:87], v[168:171], %[c9]\n" \
  "v_mfma_f32_16x16x32_bf16 %[c10], a[88:91], v[168:171], %[c10]\nv_mfma_f32_16x16x32_bf16 %[c11], a[92:95], v[168:171], %[c11]\ns_waitcnt vmcnt(17)\nv_mfma_f32_16x16x32_bf16 %[c12], a[80:83], v[172:175], %[c12]\n" \
  "v_mfma_f32_16x16x32_bf16 %[c13], a[84:87], v[172:175], %[c13]\nv_mfma_f32_16x16x32_bf16 %[c14], a[88:91], v[172:175], %[c14]\nv_mfma_f32_16x16x32_bf16 %[c15], a[92:95], v[172:175], %[c15]\n" \
  "s_waitcnt vmcnt(16)\nv_mfma_f32_16x16x32_bf16 %[c8], a[96:99], v[176:179], %[c8]\nv_mfma_f32_16x16x32_bf16 %[c9], a[100:103], v[176:179], %[c9]\nv_mfma_f32_16x16x32_bf16 %[c10], a[104:107], v[176:179], %[c10]\n" \
  "v_mfma_f32_16x16x32_bf16 %[c11], a[108:111], v[176:179], %[c11]\ns_waitcnt vmcnt(15)\nv_mfma_f32_16x16x32_bf16 %[c12], a[96:99], v[180:183], %[c12]\nv_mfma_f32_16x16x32_bf16 %[c13], a[100:103], v[180:183], %[c13]\n" \
  "v_mfma_f32_16x16x32_bf16 %[c14], a[104:107], v[180:183], %[c14]\nv_mfma_f32_16x16x32_bf16 %[c15], a[108:111], v[180:183], %[c15]\ns_waitcnt vmcnt(14)\n" \
  "v_mfma_f32_16x16x32_bf16 %[c8], a[112:115], v[184:187], %[c8]\nv_mfma_f32_16x16x32_bf16 %[c9], a[116:119], v[184:187], %[c9]\nv_mfma_f32_16x16x32_bf16 %[c10], a[120:123], v[184:187], %[c10]\n" \
  "v_mfma_f32_16x16x32_bf16 %[c11], a[124:127], v[184:187], %[c11]\ns_waitcnt vmcnt(13)\nv_mfma_f32_16x16x32_bf16 %[c12], a[112:115], v[188:191], %[c12]\n" \
  "v_mfma_f32_16x16x32_bf16 %[c13], a[116:119], v[188:191], %[c13]\nv_mfma_f32_16x16x32_bf16 %[c14], a[120:123], v[188:191], %[c14]\nv_mfma_f32_16x16x32_bf16 %[c15], a[124:127], v[188:191], %[c15]\n" \
  "s_waitcnt vmcnt(12)\nv_mfma_f32_16x16x32_bf16 %[c8], a[128:131], v[192:195], %[c8]\nv_mfma_f32_16x16x32_bf16 %[c9], a[132:135], v[192:195], %[c9]\nv_mfma_f32_16x16x32_bf16 %[c10], a[136:139], v[192:195], %[c10]\n" \
  "v_mfma_f32_16x16x32_bf16 %[c11], a[140:143], v[192:195], %[c11]\ns_waitcnt vmcnt(11)\nv_mfma_f32_16x16x32_bf16 %[c12], a[128:131], v[196:199], %[c12]\n" \
  "v_mfma_f32_16x16x32_bf16 %[c13], a[132:135], v[196:199], %[c13]\nv_mfma_f32_16x16x32_bf16 %[c14], a[136:139], v[196:199], %[c14]\nv_mfma_f32_16x16x32_bf16 %[c15], a[140:143], v[196:199], %[c15]\n" \
  "s_waitcnt vmcnt(10)\nv_mfma_f32_16x16x32_bf16 %[c8], a[144:147], v[200:203], %[c8]\nv_mfma_f32_16x16x32_bf16 %[c9], a[148:151], v[200:203], %[c9]\nv_mfma_f32_16x16x32_bf16 %[c10], a[152:155], v[200:203], %[c10]\n" \
  "v_mfma_f32_16x16x32_bf16 %[c11], a[156:159], v[200:203], %[c11]\ns_waitcnt vmcnt(9)\nv_mfma_f32_16x16x32_bf16 %[c12], a[144:147], v[204:207], %[c12]\nv_mfma_f32_16x16x32_bf16 %[c13], a[148:151], v[204:207], %[c13]\n" \
  "v_mfma_f32_16x16x32_bf16 %[c14], a[152:155], v[204:207], %[c14]\nv_mfma_f32_16x16x32_bf16 %[c15], a[156:159], v[204:207], %[c15]\ns_waitcnt vmcnt(8)\nv_mfma_f32_16x16x32_bf16 %[c8], a[160:163], v[208:211], %[c8]\n" \
  "v_mfma_f32_16x16x32_bf16 %[c9], a[164:167], v[208:211], %[c9]\nv_mfma_f32_16x16x32_bf16 %[c10], a[168:171], v[208:211], %[c10]\nv_mfma_f32_16x16x32_bf16 %[c11], a[172:175], v[208:211], %[c11]\n" \
  "s_waitcnt vmcnt(7)\nv_mfma_f32_16x16x32_bf16 %[c12], a[160:163], v[212:215], %[c12]\nv_mfma_f32_16x16x32_bf16 %[c13], a[164:167], v[212:215], %[c13]\nv_mfma_f32_16x16x32_bf16 %[c14], a[168:171], v[212:215], %[c14]\n" \
  "v_mfma_f32_16x16x32_bf16 %[c15], a[172:175], v[212:215], %[c15]\ns_waitcnt vmcnt(6)\nv_mfma_f32_16x16x32_bf16 %[c8], a[176:179], v[216:219], %[c8]\nv_mfma_f32_16x16x32_bf16 %[c9], a[180:183], v[216:219], %[c9]\n" \
  "v_mfma_f32_16x16x32_bf16 %[c10], a[184:187], v[216:219], %[c10]\nv_mfma_f32_16x16x32_bf16 %[c11], a[188:191], v[216:219], %[c11]\ns_waitcnt vmcnt(5)\nv_mfma_f32_16x16x32_bf16 %[c12], a[176:179], v[220:223], %[c12]\n" \
  "v_mfma_f32_16x16x32_bf16 %[c13], a[180:183], v[220:223], %[c13]\nv_mfma_f32_16x16x32_bf16 %[c14], a[184:187], v[220:223], %[c14]\nv_mfma_f32_16x16x32_bf16 %[c15], a[188:191], v[220:223], %[c15]\n" \
  "s_waitcnt vmcnt(4)\nv_mfma_f32_16x16x32_bf16 %[c8], a[192:195], v[224:227], %[c8]\nv_mfma_f32_16x16x32_bf16 %[c9], a[196:199], v[224:227], %[c9]\nv_mfma_f32_16x16x32_bf16 %[c10], a[200:203], v[224:227], %[c10]\n" \
  "v_mfma_f32_16x16x32_bf16 %[c11], a[204:207], v[224:227], %[c11]\ns_waitcnt vmcnt(3)\nv_mfma_f32_16x16x32_bf16 %[c12], a[192:195], v[228:231], %[c12]\nv_mfma_f32_16x16x32_bf16 %[c13], a[196:199], v[228:231], %[c13]\n" \
  "v_mfma_f32_16x16x32_bf16 %[c14], a[200:203], v[228:231], %[c14]\nv_mfma_f32_16x16x32_bf16 %[c15], a[204:207], v[228:231], %[c15]\ns_waitcnt vmcnt(2)\nv_mfma_f32_16x16x32_bf16 %[c8], a[208:211], v[232:235], %[c8]\n" \
  "v_mfma_f32_16x16x32_bf16 %[c9], a[212:215], v[232:235], %[c9]\nv_mfma_f32_16x16x32_bf16 %[c10], a[216:219], v[232:235], %[c10]\nv_mfma_f32_16x16x32_bf16 %[c11], a[220:223], v[232:235], %[c11]\n" \
  "s_waitcnt vmcnt(1)\nv_mfma_f32_16x16x32_bf16 %[c12], a[208:211], v[236:239], %[c12]\nv_mfma_f32_16x16x32_bf16 %[c13], a[212:215], v[236:239], %[c13]\nv_mfma_f32_16x16x32_bf16 %[c14], a[216:219], v[236:239], %[c14]\n" \
  "v_mfma_f32_16x16x32_bf16 %[c15], a[220:223], v[236:239], %[c15]\ns_nop 7\n"
#define R3_LOAD_ASM \
  "s_mov_b32 s93, %[lw]\ns_min_u32 s92, %[rmax], 0\ns_mul_i32 s92, s92, %[rs]\ns_mov_b32 m0, s93\ns_add_u32 s90, %[wlo], s92\ns_addc_u32 s91, %[whi], 0\nglobal_load_lds_dwordx4 %[vs], s[90:91]\n" \
  "s_add_u32 s93, s93, 1024\ns_min_u32 s92, %[rmax], 16\ns_mul_i32 s92, s92, %[rs]\ns_mov_b32 m0, s93\ns_add_u32 s90, %[wlo], s92\ns_addc_u32 s91, %[whi], 0\n" \
  "global_load_lds_dwordx4 %[vs], s[90:91]\ns_add_u32 s93, s93, 1024\ns_min_u32 s92, %[rmax], 32\ns_mul_i32 s92, s92, %[rs]\ns_mov_b32 m0, s93\ns_add_u32 s90, %[wlo], s92\n" \
  "s_addc_u32 s91, %[whi], 0\nglobal_load_lds_dwordx4 %[vs], s[90:91]\ns_add_u32 s93, s93, 1024\ns_min_u32 s92, %[rmax], 48\ns_mul_i32 s92, s92, %[rs]\ns_mov_b32 m0, s93\n" \
  "s_add_u32 s90, %[wlo], s92\ns_addc_u32 s91, %[whi], 0\nglobal_load_lds_dwordx4 %[vs], s[90:91]\ns_add_u32 s93, s93, 1024\ns_min_u32 s92, %[rmax], 64\ns_mul_i32 s92, s92, %[rs]\n" \
  "s_mov_b32 m0, s93\ns_add_u32 s90, %[wlo], s92\ns_addc_u32 s91, %[whi], 0\nglobal_load_lds_dwordx4 %[vs], s[90:91]\ns_add_u32 s93, s93, 1024\ns_min_u32 s92, %[rmax], 80\n" \
  "s_mul_i32 s92, s92, %[rs]\ns_mov_b32 m0, s93\ns_add_u32 s90, %[wlo], s92\ns_addc_u32 s91, %[whi], 0\nglobal_load_lds_dwordx4 %[vs], s[90:91]\ns_add_u32 s93, s93, 1024\n" \
  "s_min_u32 s92, %[rmax], 96\ns_mul_i32 s92, s92, %[rs]\ns_mov_b32 m0, s93\ns_add_u32 s90, %[wlo], s92\ns_addc_u32 s91, %[whi], 0\nglobal_load_lds_dwordx4 %[vs], s[90:91]\n" \
  "s_add_u32 s93, s93, 1024\ns_min_u32 s92, %[rmax], 112\ns_mul_i32 s92, s92, %[rs]\ns_mov_b32 m0, s93\ns_add_u32 s90, %[wlo], s92\ns_addc_u32 s91, %[whi], 0\n" \
  "global_load_lds_dwordx4 %[vs], s[90:91]\ns_add_u32 s93, s93, 1024\ns_min_u32 s92, %[rmax], 128\ns_mul_i32 s92, s92, %[rs]\ns_mov_b32 m0, s93\ns_add_u32 s90, %[wlo], s92\n" \
  "s_addc_u32 s91, %[whi], 0\nglobal_load_lds_dwordx4 %[vs], s[90:91]\ns_add_u32 s93, s93, 1024\ns_min_u32 s92, %[rmax], 144\ns_mul_i32 s92, s92, %[rs]\ns_mov_b32 m0, s93\n" \
  "s_add_u32 s90, %[wlo], s92\ns_addc_u32 s91, %[whi], 0\nglobal_load_lds_dwordx4 %[vs], s[90:91]\ns_add_u32 s93, s93, 1024\ns_min_u32 s92, %[rmax], 160\n" \
  "s_mul_i32 s92, s92, %[rs]\ns_mov_b32 m0, s93\ns_add_u32 s90, %[wlo], s92\ns_addc_u32 s91, %[whi], 0\nglobal_load_lds_dwordx4 %[vs], s[90:91]\ns_add_u32 s93, s93, 1024\n" \
  "s_min_u32 s92, %[rmax], 176\ns_mul_i32 s92, s92, %[rs]\ns_mov_b32 m0, s93\ns_add_u32 s90, %[wlo], s92\ns_addc_u32 s91, %[whi], 0\nglobal_load_lds_dwordx4 %[vs], s[90:91]\n" \
  "s_add_u32 s93, s93, 1024\ns_min_u32 s92, %[rmax], 192\ns_mul_i32 s92, s92, %[rs]\ns_mov_b32 m0, s93\ns_add_u32 s90, %[wlo], s92\ns_addc_u32 s91, %[whi], 0\n" \
  "global_load_lds_dwordx4 %[vs], s[90:91]\ns_add_u32 s93, s93, 1024\ns_min_u32 s92, %[rmax], 208\ns_mul_i32 s92, s92, %[rs]\ns_mov_b32 m0, s93\ns_add_u32 s90, %[wlo], s92\n" \
  "s_addc_u32 s91, %[whi], 0\nglobal_load_lds_dwordx4 %[vs], s[90:91]\ns_add_u32 s93, s93, 1024\ns_min_u32 s92, %[rmax], 224\ns_mul_i32 s92, s92, %[rs]\ns_mov_b32 m0, s93\n" \
  "s_add_u32 s90, %[wlo], s92\ns_addc_u32 s91, %[whi], 0\nglobal_load_lds_dwordx4 %[vs], s[90:91]\ns_add_u32 s93, s93, 1024\ns_min_u32 s92, %[rmax], 240\n" \
  "s_mul_i32 s92, s92, %[rs]\ns_mov_b32 m0, s93\ns_add_u32 s90, %[wlo], s92\ns_addc_u32 s91, %[whi], 0\nglobal_load_lds_dwordx4 %[vs], s[90:91]\ns_add_u32 s93, s93, 1024\n" \
  "s_min_u32 s92, %[rmax], 256\ns_mul_i32 s92, s92, %[rs]\ns_mov_b32 m0, s93\ns_add_u32 s90, %[wlo], s92\ns_addc_u32 s91, %[whi], 0\nglobal_load_lds_dwordx4 %[vs], s[90:91]\n" \
  "s_add_u32 s93, s93, 1024\ns_min_u32 s92, %[rmax], 272\ns_mul_i32 s92, s92, %[rs]\ns_mov_b32 m0, s93\ns_add_u32 s90, %[wlo], s92\ns_addc_u32 s91, %[whi], 0\n" \
  "global_load_lds_dwordx4 %[vs], s[90:91]\ns_add_u32 s93, s93, 1024\ns_min_u32 s92, %[rmax], 288\ns_mul_i32 s92, s92, %[rs]\ns_mov_b32 m0, s93\ns_add_u32 s90, %[wlo], s92\n" \
  "s_addc_u32 s91, %[whi], 0\nglobal_load_lds_dwordx4 %[vs], s[90:91]\ns_add_u32 s93, s93, 1024\ns_min_u32 s92, %[rmax], 304\ns_mul_i32 s92, s92, %[rs]\ns_mov_b32 m0, s93\n" \
  "s_add_u32 s90, %[wlo], s92\ns_addc_u32 s91, %[whi], 0\nglobal_load_lds_dwordx4 %[vs], s[90:91]\ns_add_u32 s93, s93, 1024\ns_min_u32 s92, %[rmax], 320\n" \
  "s_mul_i32 s92, s92, %[rs]\ns_mov_b32 m0, s93\ns_add_u32 s90, %[wlo], s92\ns_addc_u32 s91, %[whi], 0\nglobal_load_lds_dwordx4 %[vs], s[90:91]\ns_add_u32 s93, s93, 1024\n" \
  "s_min_u32 s92, %[rmax], 336\ns_mul_i32 s92, s92, %[rs]\ns_mov_b32 m0, s93\ns_add_u32 s90, %[wlo], s92\ns_addc_u32 s91, %[whi], 0\nglobal_load_lds_dwordx4 %[vs], s[90:91]\n" \
  "s_add_u32 s93, s93, 1024\ns_min_u32 s92, %[rmax], 352\ns_mul_i32 s92, s92, %[rs]\ns_mov_b32 m0, s93\ns_add_u32 s90, %[wlo], s92\ns_addc_u32 s91, %[whi], 0\n" \
  "global_load_lds_dwordx4 %[vs], s[90:91]\ns_add_u32 s93, s93, 1024\ns_min_u32 s92, %[rmax], 368\ns_mul_i32 s92, s92, %[rs]\ns_mov_b32 m0, s93\ns_add_u32 s90, %[wlo], s92\n" \
  "s_addc_u32 s91, %[whi], 0\nglobal_load_lds_dwordx4 %[vs], s[90:91]\ns_add_u32 s93, s93, 1024\ns_min_u32 s92, %[rmax], 384\ns_mul_i32 s92, s92, %[rs]\ns_mov_b32 m0, s93\n" \
  "s_add_u32 s90, %[wlo], s92\ns_addc_u32 s91, %[whi], 0\nglobal_load_lds_dwordx4 %[vs], s[90:91]\ns_add_u32 s93, s93, 1024\ns_min_u32 s92, %[rmax], 400\n" \
  "s_mul_i32 s92, s92, %[rs]\ns_mov_b32 m0, s93\ns_add_u32 s90, %[wlo], s92\ns_addc_u32 s91, %[whi], 0\nglobal_load_lds_dwordx4 %[vs], s[90:91]\ns_add_u32 s93, s93, 1024\n" \
  "s_min_u32 s92, %[rmax], 416\ns_mul_i32 s92, s92, %[rs]\ns_mov_b32 m0, s93\ns_add_u32 s90, %[wlo], s92\ns_addc_u32 s91, %[whi], 0\nglobal_load_lds_dwordx4 %[vs], s[90:91]\n" \
  "s_add_u32 s93, s93, 1024\ns_min_u32 s92, %[rmax], 432\ns_mul_i32 s92, s92, %[rs]\ns_mov_b32 m0, s93\ns_add_u32 s90, %[wlo], s92\ns_addc_u32 s91, %[whi], 0\n" \
  "global_load_lds_dwordx4 %[vs], s[90:91]\ns_add_u32 s93, s93, 1024\ns_min_u32 s92, %[rmax], 448\ns_mul_i32 s92, s92, %[rs]\ns_mov_b32 m0, s93\ns_add_u32 s90, %[wlo], s92\n" \
  "s_addc_u32 s91, %[whi], 0\nglobal_load_lds_dwordx4 %[vs], s[90:91]\ns_add_u32 s93, s93, 1024\ns_min_u32 s92, %[rmax], 464\ns_mul_i32 s92, s92, %[rs]\ns_mov_b32 m0, s93\n" \
  "s_add_u32 s90, %[wlo], s92\ns_addc_u32 s91, %[whi], 0\nglobal_load_lds_dwordx4 %[vs], s[90:91]\ns_add_u32 s93, s93, 1024\ns_min_u32 s92, %[rmax], 480\n" \
  "s_mul_i32 s92, s92, %[rs]\ns_mov_b32 m0, s93\ns_add_u32 s90, %[wlo], s92\ns_addc_u32 s91, %[whi], 0\nglobal_load_lds_dwordx4 %[vs], s[90:91]\ns_add_u32 s93, s93, 1024\n" \
  "s_min_u32 s92, %[rmax], 496\ns_mul_i32 s92, s92, %[rs]\ns_mov_b32 m0, s93\ns_add_u32 s90, %[wlo], s92\ns_addc_u32 s91, %[whi], 0\nglobal_load_lds_dwordx4 %[vs], s[90:91]\n" \
  "s_add_u32 s93, s93, 1024\ns_waitcnt vmcnt(0)\nds_read_b64_tr_b16 a[0:1], %[t0] offset:0\nds_read_b64_tr_b16 a[2:3], %[t0] offset:1024\nds_read_b64_tr_b16 a[4:5], %[t1] offset:0\n" \
  "ds_read_b64_tr_b16 a[6:7], %[t1] offset:1024\nds_read_b64_tr_b16 a[8:9], %[t0] offset:2048\nds_read_b64_tr_b16 a[10:11], %[t0] offset:3072\nds_read_b64_tr_b16 a[12:13], %[t1] offset:2048\n" \
  "ds_read_b64_tr_b16 a[14:15], %[t1] offset:3072\nds_read_b64_tr_b16 a[16:17], %[t0] offset:4096\nds_read_b64_tr_b16 a[18:19], %[t0] offset:5120\nds_read_b64_tr_b16 a[20:21], %[t1] offset:4096\n" \
  "ds_read_b64_tr_b16 a[22:23], %[t1] offset:5120\ns_waitcnt lgkmcnt(4)\nds_read_b64_tr_b16 a[24:25], %[t0] offset:6144\nds_read_b64_tr_b16 a[26:27], %[t0] offset:7168\n" \
  "ds_read_b64_tr_b16 a[28:29], %[t1] offset:6144\nds_read_b64_tr_b16 a[30:31], %[t1] offset:7168\nds_read_b64_tr_b16 a[32:33], %[t0] offset:8192\nds_read_b64_tr_b16 a[34:35], %[t0] offset:9216\n" \
  "ds_read_b64_tr_b16 a[36:37], %[t1] offset:8192\nds_read_b64_tr_b16 a[38:39], %[t1] offset:9216\nds_read_b64_tr_b16 a[40:41], %[t0] offset:10240\nds_read_b64_tr_b16 a[42:43], %[t0] offset:11264\n" \
  "ds_read_b64_tr_b16 a[44:45], %[t1] offset:10240\nds_read_b64_tr_b16 a[46:47], %[t1] offset:11264\ns_waitcnt lgkmcnt(4)\nds_read_b64_tr_b16 a[48:49], %[t0] offset:12288\n" \
  "ds_read_b64_tr_b16 a[50:51], %[t0] offset:13312\nds_read_b64_tr_b16 a[52:53], %[t1] offset:12288\nds_read_b64_tr_b16 a[54:55], %[t1] offset:13312\nds_read_b64_tr_b16 a[56:57], %[t0] offset:14336\n" \
  "ds_read_b64_tr_b16 a[58:59], %[t0] offset:15360\nds_read_b64_tr_b16 a[60:61], %[t1] offset:14336\nds_read_b64_tr_b16 a[62:63], %[t1] offset:15360\nds_read_b64_tr_b16 a[64:65], %[t0] offset:16384\n" \
  "ds_read_b64_tr_b16 a[66:67], %[t0] offset:17408\nds_read_b64_tr_b16 a[68:69], %[t1] offset:16384\nds_read_b64_tr_b16 a[70:71], %[t1] offset:17408\ns_waitcnt lgkmcnt(4)\n" \
  "ds_read_b64_tr_b16 a[72:73], %[t0] offset:18432\nds_read_b64_tr_b16 a[74:75], %[t0] offset:19456\nds_read_b64_tr_b16 a[76:77], %[t1] offset:18432\nds_read_b64_tr_b16 a[78:79], %[t1] offset:19456\n" \
  "ds_read_b64_tr_b16 a[80:81], %[t0] offset:20480\nds_read_b64_tr_b16 a[82:83], %[t0] offset:21504\nds_read_b64_tr_b16 a[84:85], %[t1] offset:20480\nds_read_b64_tr_b16 a[86:87], %[t1] offset:21504\n" \
  "ds_read_b64_tr_b16 a[88:89], %[t0] offset:22528\nds_read_b64_tr_b16 a[90:91], %[t0] offset:23552\nds_read_b64_tr_b16 a[92:93], %[t1] offset:22528\nds_read_b64_tr_b16 a[94:95], %[t1] offset:23552\n" \
  "s_waitcnt lgkmcnt(4)\nds_read_b64_tr_b16 a[96:97], %[t0] offset:24576\nds_read_b64_tr_b16 a[98:99], %[t0] offset:25600\nds_read_b64_tr_b16 a[100:101], %[t1] offset:24576\n" \
  "ds_read_b64_tr_b16 a[102:103], %[t1] offset:25600\nds_read_b64_tr_b16 a[104:105], %[t0] offset:26624\nds_read_b64_tr_b16 a[106:107], %[t0] offset:27648\n" \
  "ds_read_b64_tr_b16 a[108:109], %[t1] offset:26624\nds_read_b64_tr_b16 a[110:111], %[t1] offset:27648\nds_read_b64_tr_b16 a[112:113], %[t0] offset:28672\n" \
  "ds_read_b64_tr_b16 a[114:115], %[t0] offset:29696\nds_read_b64_tr_b16 a[116:117], %[t1] offset:28672\nds_read_b64_tr_b16 a[118:119], %[t1] offset:29696\n" \
  "s_waitcnt lgkmcnt(4)\nds_read_b64_tr_b16 a[120:121], %[t0] offset:30720\nds_read_b64_tr_b16 a[122:123], %[t0] offset:31744\nds_read_b64_tr_b16 a[124:125], %[t1] offset:30720\n" \
  "ds_read_b64_tr_b16 a[126:127], %[t1] offset:31744\ns_waitcnt lgkmcnt(0)\ns_mov_b32 s93, %[lw]\ns_min_u32 s92, %[rmax], 512\ns_mul_i32 s92, s92, %[rs]\n" \
  "s_mov_b32 m0, s93\ns_add_u32 s90, %[wlo], s92\ns_addc_u32 s91, %[whi], 0\nglobal_load_lds_dwordx4 %[vs], s[90:91]\ns_add_u32 s93, s93, 1024\ns_min_u32 s92, %[rmax], 528\n" \
  "s_mul_i32 s92, s92, %[rs]\ns_mov_b32 m0, s93\ns_add_u32 s90, %[wlo], s92\ns_addc_u32 s91, %[whi], 0\nglobal_load_lds_dwordx4 %[vs], s[90:91]\ns_add_u32 s93, s93, 1024\n" \
  "s_min_u32 s92, %[rmax], 544\ns_mul_i32 s92, s92, %[rs]\ns_mov_b32 m0, s93\ns_add_u32 s90, %[wlo], s92\ns_addc_u32 s91, %[whi], 0\nglobal_load_lds_dwordx4 %[vs], s[90:91]\n" \
  "s_add_u32 s93, s93, 1024\ns_min_u32 s92, %[rmax], 560\ns_mul_i32 s92, s92, %[rs]\ns_mov_b32 m0, s93\ns_add_u32 s90, %[wlo], s92\ns_addc_u32 s91, %[whi], 0\n" \
  "global_load_lds_dwordx4 %[vs], s[90:91]\ns_add_u32 s93, s93, 1024\ns_min_u32 s92, %[rmax], 576\ns_mul_i32 s92, s92, %[rs]\ns_mov_b32 m0, s93\ns_add_u32 s90, %[wlo], s92\n" \
  "s_addc_u32 s91, %[whi], 0\nglobal_load_lds_dwordx4 %[vs], s[90:91]\ns_add_u32 s93, s93, 1024\ns_min_u32 s92, %[rmax], 592\ns_mul_i32 s92, s92, %[rs]\ns_mov_b32 m0, s93\n" \
  "s_add_u32 s90, %[wlo], s92\ns_addc_u32 s91, %[whi], 0\nglobal_load_lds_dwordx4 %[vs], s[90:91]\ns_add_u32 s93, s93, 1024\ns_min_u32 s92, %[rmax], 608\n" \
  "s_mul_i32 s92, s92, %[rs]\ns_mov_b32 m0, s93\ns_add_u32 s90, %[wlo], s92\ns_addc_u32 s91, %[whi], 0\nglobal_load_lds_dwordx4 %[vs], s[90:91]\ns_add_u32 s93, s93, 1024\n" \
  "s_min_u32 s92, %[rmax], 624\ns_mul_i32 s92, s92, %[rs]\ns_mov_b32 m0, s93\ns_add_u32 s90, %[wlo], s92\ns_addc_u32 s91, %[whi], 0\nglobal_load_lds_dwordx4 %[vs], s[90:91]\n" \
  "s_add_u32 s93, s93, 1024\ns_min_u32 s92, %[rmax], 640\ns_mul_i32 s92, s92, %[rs]\ns_mov_b32 m0, s93\ns_add_u32 s90, %[wlo], s92\ns_addc_u32 s91, %[whi], 0\n" \
  "global_load_lds_dwordx4 %[vs], s[90:91]\ns_add_u32 s93, s93, 1024\ns_min_u32 s92, %[rmax], 656\ns_mul_i32 s92, s92, %[rs]\ns_mov_b32 m0, s93\ns_add_u32 s90, %[wlo], s92\n" \
  "s_addc_u32 s91, %[whi], 0\nglobal_load_lds_dwordx4 %[vs], s[90:91]\ns_add_u32 s93, s93, 1024\ns_min_u32 s92, %[rmax], 672\ns_mul_i32 s92, s92, %[rs]\ns_mov_b32 m0, s93\n" \
  "s_add_u32 s90, %[wlo], s92\ns_addc_u32 s91, %[whi], 0\nglobal_load_lds_dwordx4 %[vs], s[90:91]\ns_add_u32 s93, s93, 1024\ns_min_u32 s92, %[rmax], 688\n" \
  "s_mul_i32 s92, s92, %[rs]\ns_mov_b32 m0, s93\ns_add_u32 s90, %[wlo], s92\ns_addc_u32 s91, %[whi], 0\nglobal_load_lds_dwordx4 %[vs], s[90:91]\ns_add_u32 s93, s93, 1024\n" \
  "s_min_u32 s92, %[rmax], 704\ns_mul_i32 s92, s92, %[rs]\ns_mov_b32 m0, s93\ns_add_u32 s90, %[wlo], s92\ns_addc_u32 s91, %[whi], 0\nglobal_load_lds_dwordx4 %[vs], s[90:91]\n" \
  "s_add_u32 s93, s93, 1024\ns_min_u32 s92, %[rmax], 720\ns_mul_i32 s92, s92, %[rs]\ns_mov_b32 m0, s93\ns_add_u32 s90, %[wlo], s92\ns_addc_u32 s91, %[whi], 0\n" \
  "global_load_lds_dwordx4 %[vs], s[90:91]\ns_add_u32 s93, s93, 1024\ns_min_u32 s92, %[rmax], 736\ns_mul_i32 s92, s92, %[rs]\ns_mov_b32 m0, s93\ns_add_u32 s90, %[wlo], s92\n" \
  "s_addc_u32 s91, %[whi], 0\nglobal_load_lds_dwordx4 %[vs], s[90:91]\ns_add_u32 s93, s93, 1024\ns_min_u32 s92, %[rmax], 752\ns_mul_i32 s92, s92, %[rs]\ns_mov_b32 m0, s93\n" \
  "s_add_u32 s90, %[wlo], s92\ns_addc_u32 s91, %[whi], 0\nglobal_load_lds_dwordx4 %[vs], s[90:91]\ns_add_u32 s93, s93, 1024\ns_min_u32 s92, %[rmax], 768\n" \
  "s_mul_i32 s92, s92, %[rs]\ns_mov_b32 m0, s93\ns_add_u32 s90, %[wlo], s92\ns_addc_u32 s91, %[whi], 0\nglobal_load_lds_dwordx4 %[vs], s[90:91]\ns_add_u32 s93, s93, 1024\n" \
  "s_min_u32 s92, %[rmax], 784\ns_mul_i32 s92, s92, %[rs]\ns_mov_b32 m0, s93\ns_add_u32 s90, %[wlo], s92\ns_addc_u32 s91, %[whi], 0\nglobal_load_lds_dwordx4 %[vs], s[90:91]\n" \
  "s_add_u32 s93, s93, 1024\ns_min_u32 s92, %[rmax], 800\ns_mul_i32 s92, s92, %[rs]\ns_mov_b32 m0, s93\ns_add_u32 s90, %[wlo], s92\ns_addc_u32 s91, %[whi], 0\n" \
  "global_load_lds_dwordx4 %[vs], s[90:91]\ns_add_u32 s93, s93, 1024\ns_min_u32 s92, %[rmax], 816\ns_mul_i32 s92, s92, %[rs]\ns_mov_b32 m0, s93\ns_add_u32 s90, %[wlo], s92\n" \
  "s_addc_u32 s91, %[whi], 0\nglobal_load_lds_dwordx4 %[vs], s[90:91]\ns_add_u32 s93, s93, 1024\ns_min_u32 s92, %[rmax], 832\ns_mul_i32 s92, s92, %[rs]\ns_mov_b32 m0, s93\n" \
  "s_add_u32 s90, %[wlo], s92\ns_addc_u32 s91, %[whi], 0\nglobal_load_lds_dwordx4 %[vs], s[90:91]\ns_add_u32 s93, s93, 1024\ns_min_u32 s92, %[rmax], 848\n" \
  "s_mul_i32 s92, s92, %[rs]\ns_mov_b32 m0, s93\ns_add_u32 s90, %[wlo], s92\ns_addc_u32 s91, %[whi], 0\nglobal_load_lds_dwordx4 %[vs], s[90:91]\ns_add_u32 s93, s93, 1024\n" \
  "s_min_u32 s92, %[rmax], 864\ns_mul_i32 s92, s92, %[rs]\ns_mov_b32 m0, s93\ns_add_u32 s90, %[wlo], s92\ns_addc_u32 s91, %[whi], 0\nglobal_load_lds_dwordx4 %[vs], s[90:91]\n" \
  "s_add_u32 s93, s93, 1024\ns_min_u32 s92, %[rmax], 880\ns_mul_i32 s92, s92, %[rs]\ns_mov_b32 m0, s93\ns_add_u32 s90, %[wlo], s92\ns_addc_u32 s91, %[whi], 0\n" \
  "global_load_lds_dwordx4 %[vs], s[90:91]\ns_add_u32 s93, s93, 1024\ns_min_u32 s92, %[rmax], 896\ns_mul_i32 s92, s92, %[rs]\ns_mov_b32 m0, s93\ns_add_u32 s90, %[wlo], s92\n" \
  "s_addc_u32 s91, %[whi], 0\nglobal_load_lds_dwordx4 %[vs], s[90:91]\ns_add_u32 s93, s93, 1024\ns_min_u32 s92, %[rmax], 912\ns_mul_i32 s92, s92, %[rs]\ns_mov_b32 m0, s93\n" \
  "s_add_u32 s90, %[wlo], s92\ns_addc_u32 s91, %[whi], 0\nglobal_load_lds_dwordx4 %[vs], s[90:91]\ns_add_u32 s93, s93, 1024\ns_min_u32 s92, %[rmax], 928\n" \
  "s_mul_i32 s92, s92, %[rs]\ns_mov_b32 m0, s93\ns_add_u32 s90, %[wlo], s92\ns_addc_u32 s91, %[whi], 0\nglobal_load_lds_dwordx4 %[vs], s[90:91]\ns_add_u32 s93, s93, 1024\n" \
  "s_min_u32 s92, %[rmax], 944\ns_mul_i32 s92, s92, %[rs]\ns_mov_b32 m0, s93\ns_add_u32 s90, %[wlo], s92\ns_addc_u32 s91, %[whi], 0\nglobal_load_lds_dwordx4 %[vs], s[90:91]\n" \
  "s_add_u32 s93, s93, 1024\ns_min_u32 s92, %[rmax], 960\ns_mul_i32 s92, s92, %[rs]\ns_mov_b32 m0, s93\ns_add_u32 s90, %[wlo], s92\ns_addc_u32 s91, %[whi], 0\n" \
  "global_load_lds_dwordx4 %[vs], s[90:91]\ns_add_u32 s93, s93, 1024\ns_min_u32 s92, %[rmax], 976\ns_mul_i32 s92, s92, %[rs]\ns_mov_b32 m0, s93\ns_add_u32 s90, %[wlo], s92\n" \
  "s_addc_u32 s91, %[whi], 0\nglobal_load_lds_dwordx4 %[vs], s[90:91]\ns_add_u32 s93, s93, 1024\ns_min_u32 s92, %[rmax], 992\ns_mul_i32 s92, s92, %[rs]\ns_mov_b32 m0, s93\n" \
  "s_add_u32 s90, %[wlo], s92\ns_addc_u32 s91, %[whi], 0\nglobal_load_lds_dwordx4 %[vs], s[90:91]\ns_add_u32 s93, s93, 1024\ns_min_u32 s92, %[rmax], 1008\n" \
  "s_mul_i32 s92, s92, %[rs]\ns_mov_b32 m0, s93\ns_add_u32 s90, %[wlo], s92\ns_addc_u32 s91, %[whi], 0\nglobal_load_lds_dwordx4 %[vs], s[90:91]\ns_add_u32 s93, s93, 1024\n" \
  "s_waitcnt vmcnt(0)\nds_read_b64_tr_b16 a[128:129], %[t0] offset:0\nds_read_b64_tr_b16 a[130:131], %[t0] offset:1024\nds_read_b64_tr_b16 a[132:133], %[t1] offset:0\n" \
  "ds_read_b64_tr_b16 a[134:135], %[t1] offset:1024\nds_read_b64_tr_b16 a[136:137], %[t0] offset:2048\nds_read_b64_tr_b16 a[138:139], %[t0] offset:3072\nds_read_b64_tr_b16 a[140:141], %[t1] offset:2048\n" \
  "ds_read_b64_tr_b16 a[142:143], %[t1] offset:3072\nds_read_b64_tr_b16 a[144:145], %[t0] offset:4096\nds_read_b64_tr_b16 a[146:147], %[t0] offset:5120\nds_read_b64_tr_b16 a[148:149], %[t1] offset:4096\n" \
  "ds_read_b64_tr_b16 a[150:151], %[t1] offset:5120\ns_waitcnt lgkmcnt(4)\nds_read_b64_tr_b16 a[152:153], %[t0] offset:6144\nds_read_b64_tr_b16 a[154:155], %[t0] offset:7168\n" \
  "ds_read_b64_tr_b16 a[156:157], %[t1] offset:6144\nds_read_b64_tr_b16 a[158:159], %[t1] offset:7168\nds_read_b64_tr_b16 a[160:161], %[t0] offset:8192\nds_read_b64_tr_b16 a[162:163], %[t0] offset:9216\n" \
  "ds_read_b64_tr_b16 a[164:165], %[t1] offset:8192\nds_read_b64_tr_b16 a[166:167], %[t1] offset:9216\nds_read_b64_tr_b16 a[168:169], %[t0] offset:10240\n" \
  "ds_read_b64_tr_b16 a[170:171], %[t0] offset:11264\nds_read_b64_tr_b16 a[172:173], %[t1] offset:10240\nds_read_b64_tr_b16 a[174:175], %[t1] offset:11264\n" \
  "s_waitcnt lgkmcnt(4)\nds_read_b64_tr_b16 a[176:177], %[t0] offset:12288\nds_read_b64_tr_b16 a[178:179], %[t0] offset:13312\nds_read_b64_tr_b16 a[180:181], %[t1] offset:12288\n" \
  "ds_read_b64_tr_b16 a[182:183], %[t1] offset:13312\nds_read_b64_tr_b16 a[184:185], %[t0] offset:14336\nds_read_b64_tr_b16 a[186:187], %[t0] offset:15360\n" \
  "ds_read_b64_tr_b16 a[188:189], %[t1] offset:14336\nds_read_b64_tr_b16 a[190:191], %[t1] offset:15360\nds_read_b64_tr_b16 a[192:193], %[t0] offset:16384\n" \
  "ds_read_b64_tr_b16 a[194:195], %[t0] offset:17408\nds_read_b64_tr_b16 a[196:197], %[t1] offset:16384\nds_read_b64_tr_b16 a[198:199], %[t1] offset:17408\n" \
  "s_waitcnt lgkmcnt(4)\nds_read_b64_tr_b16 a[200:201], %[t0] offset:18432\nds_read_b64_tr_b16 a[202:203], %[t0] offset:19456\nds_read_b64_tr_b16 a[204:205], %[t1] offset:18432\n" \
  "ds_read_b64_tr_b16 a[206:207], %[t1] offset:19456\nds_read_b64_tr_b16 a[208:209], %[t0] offset:20480\nds_read_b64_tr_b16 a[210:211], %[t0] offset:21504\n" \
  "ds_read_b64_tr_b16 a[212:213], %[t1] offset:20480\nds_read_b64_tr_b16 a[214:215], %[t1] offset:21504\nds_read_b64_tr_b16 a[216:217], %[t0] offset:22528\n" \
  "ds_read_b64_tr_b16 a[218:219], %[t0] offset:23552\nds_read_b64_tr_b16 a[220:221], %[t1] offset:22528\nds_read_b64_tr_b16 a[222:223], %[t1] offset:23552\n" \
  "s_waitcnt lgkmcnt(4)\nds_read_b64_tr_b16 a[224:225], %[t0] offset:24576\nds_read_b64_tr_b16 a[226:227], %[t0] offset:25600\nds_read_b64_tr_b16 a[228:229], %[t1] offset:24576\n" \
  "ds_read_b64_tr_b16 a[230:231], %[t1] offset:25600\nds_read_b64_tr_b16 a[232:233], %[t0] offset:26624\nds_read_b64_tr_b16 a[234:235], %[t0] offset:27648\n" \
  "ds_read_b64_tr_b16 a[236:237], %[t1] offset:26624\nds_read_b64_tr_b16 a[238:239], %[t1] offset:27648\nds_read_b64_tr_b16 a[240:241], %[t0] offset:28672\n" \
  "ds_read_b64_tr_b16 a[242:243], %[t0] offset:29696\nds_read_b64_tr_b16 a[244:245], %[t1] offset:28672\nds_read_b64_tr_b16 a[246:247], %[t1] offset:29696\n" \
  "s_waitcnt lgkmcnt(4)\nds_read_b64_tr_b16 a[248:249], %[t0] offset:30720\nds_read_b64_tr_b16 a[250:251], %[t0] offset:31744\nds_read_b64_tr_b16 a[252:253], %[t1] offset:30720\n" \
  "ds_read_b64_tr_b16 a[254:255], %[t1] offset:31744\ns_waitcnt lgkmcnt(0)\n"
#define R3_MAIN_ASM \
  "s_mov_b64 s[90:91], %[src]\nglobal_load_dwordx4 v[128:131], %[l16], s[90:91] offset:-4096 sc1\nglobal_load_dwordx4 v[132:135], %[l16], s[90:91] offset:-3072 sc1\n" \
  "global_load_dwordx4 v[136:139], %[l16], s[90:91] offset:-2048 sc1\nglobal_load_dwordx4 v[140:143], %[l16], s[90:91] offset:-1024 sc1\nglobal_load_dwordx4 v[144:147], %[l16], s[90:91] offset:0 sc1\n" \
  "global_load_dwordx4 v[148:151], %[l16], s[90:91] offset:1024 sc1\nglobal_load_dwordx4 v[152:155], %[l16], s[90:91] offset:2048 sc1\nglobal_load_dwordx4 v[156:159], %[l16], s[90:91] offset:3072 sc1\n" \
  "s_add_u32 s90, s90, 16384\ns_addc_u32 s91, s91, 0\nglobal_load_dwordx4 v[160:163], %[l16], s[90:91] offset:-4096 sc1\nglobal_load_dwordx4 v[164:167], %[l16], s[90:91] offset:-3072 sc1\n" \
  "global_load_dwordx4 v[168:171], %[l16], s[90:91] offset:-2048 sc1\nglobal_load_dwordx4 v[172:175], %[l16], s[90:91] offset:-1024 sc1\nglobal_load_dwordx4 v[176:179], %[l16], s[90:91] offset:0 sc1\n" \
  "global_load_dwordx4 v[180:183], %[l16], s[90:91] offset:1024 sc1\nglobal_load_dwordx4 v[184:187], %[l16], s[90:91] offset:2048 sc1\nglobal_load_dwordx4 v[188:191], %[l16], s[90:91] offset:3072 sc1\n" \
  "s_add_u32 s90, s90, 16384\ns_addc_u32 s91, s91, 0\nglobal_load_dwordx4 v[192:195], %[l16], s[90:91] offset:-4096 sc1\nglobal_load_dwordx4 v[196:199], %[l16], s[90:91] offset:-3072 sc1\n" \
  "global_load_dwordx4 v[200:203], %[l16], s[90:91] offset:-2048 sc1\nglobal_load_dwordx4 v[204:207], %[l16], s[90:91] offset:-1024 sc1\nglobal_load_dwordx4 v[208:211], %[l16], s[90:91] offset:0 sc1\n" \
  "global_load_dwordx4 v[212:215], %[l16], s[90:91] offset:1024 sc1\nglobal_load_dwordx4 v[216:219], %[l16], s[90:91] offset:2048 sc1\nglobal_load_dwordx4 v[220:223], %[l16], s[90:91] offset:3072 sc1\n" \
  "s_waitcnt vmcnt(23)\nv_mfma_f32_16x16x32_bf16 %[c0], a[0:3], v[128:131], 0\nv_mfma_f32_16x16x32_bf16 %[c1], a[4:7], v[128:131], 0\ns_add_u32 s90, s90, 16384\n" \
  "s_addc_u32 s91, s91, 0\nglobal_load_dwordx4 v[128:131], %[l16], s[90:91] offset:-4096 sc1\ns_waitcnt vmcnt(23)\nv_mfma_f32_16x16x32_bf16 %[c2], a[0:3], v[132:135], 0\n" \
  "v_mfma_f32_16x16x32_bf16 %[c3], a[4:7], v[132:135], 0\nglobal_load_dwordx4 v[132:135], %[l16], s[90:91] offset:-3072 sc1\ns_waitcnt vmcnt(23)\nv_mfma_f32_16x16x32_bf16 %[c0], a[8:11], v[136:139], %[c0]\n" \
  "v_mfma_f32_16x16x32_bf16 %[c1], a[12:15], v[136:139], %[c1]\nglobal_load_dwordx4 v[136:139], %[l16], s[90:91] offset:-2048 sc1\ns_waitcnt vmcnt(23)\nv_mfma_f32_16x16x32_bf16 %[c2], a[8:11], v[140:143], %[c2]\n" \
  "v_mfma_f32_16x16x32_bf16 %[c3], a[12:15], v[140:143], %[c3]\nglobal_load_dwordx4 v[140:143], %[l16], s[90:91] offset:-1024 sc1\ns_waitcnt vmcnt(23)\nv_mfma_f32_16x16x32_bf16 %[c0], a[16:19], v[144:147], %[c0]\n" \
  "v_mfma_f32_16x16x32_bf16 %[c1], a[20:23], v[144:147], %[c1]\nglobal_load_dwordx4 v[144:147], %[l16], s[90:91] offset:0 sc1\ns_waitcnt vmcnt(23)\nv_mfma_f32_16x16x32_bf16 %[c2], a[16:19], v[148:151], %[c2]\n" \
  "v_mfma_f32_16x16x32_bf16 %[c3], a[20:23], v[148:151], %[c3]\nglobal_load_dwordx4 v[148:151], %[l16], s[90:91] offset:1024 sc1\ns_waitcnt vmcnt(23)\nv_mfma_f32_16x16x32_bf16 %[c0], a[24:27], v[152:155], %[c0]\n" \
  "v_mfma_f32_16x16x32_bf16 %[c1], a[28:31], v[152:155], %[c1]\nglobal_load_dwordx4 v[152:155], %[l16], s[90:91] offset:2048 sc1\ns_waitcnt vmcnt(23)\nv_mfma_f32_16x16x32_bf16 %[c2], a[24:27], v[156:159], %[c2]\n" \
  "v_mfma_f32_16x16x32_bf16 %[c3], a[28:31], v[156:159], %[c3]\nglobal_load_dwordx4 v[156:159], %[l16], s[90:91] offset:3072 sc1\ns_waitcnt vmcnt(23)\nv_mfma_f32_16x16x32_bf16 %[c0], a[32:35], v[160:163], %[c0]\n" \
  "v_mfma_f32_16x16x32_bf16 %[c1], a[36:39], v[160:163], %[c1]\ns_add_u32 s90, s90, 16384\ns_addc_u32 s91, s91, 0\nglobal_load_dwordx4 v[160:163], %[l16], s[90:91] offset:-4096 sc1\n" \
  "s_waitcnt vmcnt(23)\nv_mfma_f32_16x16x32_bf16 %[c2], a[32:35], v[164:167], %[c2]\nv_mfma_f32_16x16x32_bf16 %[c3], a[36:39], v[164:167], %[c3]\nglobal_load_dwordx4 v[164:167], %[l16], s[90:91] offset:-3072 sc1\n" \
  "s_waitcnt vmcnt(23)\nv_mfma_f32_16x16x32_bf16 %[c0], a[40:43], v[168:171], %[c0]\nv_mfma_f32_16x16x32_bf16 %[c1], a[44:47], v[168:171], %[c1]\nglobal_load_dwordx4 v[168:171], %[l16], s[90:91] offset:-2048 sc1\n" \
  "s_waitcnt vmcnt(23)\nv_mfma_f32_16x16x32_bf16 %[c2], a[40:43], v[172:175], %[c2]\nv_mfma_f32_16x16x32_bf16 %[c3], a[44:47], v[172:175], %[c3]\nglobal_load_dwordx4 v[172:175], %[l16], s[90:91] offset:-1024 sc1\n" \
  "s_waitcnt vmcnt(23)\nv_mfma_f32_16x16x32_bf16 %[c0], a[48:51], v[176:179], %[c0]\nv_mfma_f32_16x16x32_bf16 %[c1], a[52:55], v[176:179], %[c1]\nglobal_load_dwordx4 v[176:179], %[l16], s[90:91] offset:0 sc1\n" \
  "s_waitcnt vmcnt(23)\nv_mfma_f32_16x16x32_bf16 %[c2], a[48:51], v[180:183], %[c2]\nv_mfma_f32_16x16x32_bf16 %[c3], a[52:55], v[180:183], %[c3]\nglobal_load_dwordx4 v[180:183], %[l16], s[90:91] offset:1024 sc1\n" \
  "s_waitcnt vmcnt(23)\nv_mfma_f32_16x16x32_bf16 %[c0], a[56:59], v[184:187], %[c0]\nv_mfma_f32_16x16x32_bf16 %[c1], a[60:63], v[184:187], %[c1]\nglobal_load_dwordx4 v[184:187], %[l16], s[90:91] offset:2048 sc1\n" \
  "s_waitcnt vmcnt(23)\nv_mfma_f32_16x16x32_bf16 %[c2], a[56:59], v[188:191], %[c2]\nv_mfma_f32_16x16x32_bf16 %[c3], a[60:63], v[188:191], %[c3]\nglobal_load_dwordx4 v[188:191], %[l16], s[90:91] offset:3072 sc1\n" \
  "s_waitcnt vmcnt(23)\nv_mfma_f32_16x16x32_bf16 %[c0], a[64:67], v[192:195], %[c0]\nv_mfma_f32_16x16x32_bf16 %[c1], a[68:71], v[192:195], %[c1]\ns_add_u32 s90, s90, 16384\n" \
  "s_addc_u32 s91, s91, 0\nglobal_load_dwordx4 v[192:195], %[l16], s[90:91] offset:-4096 sc1\ns_waitcnt vmcnt(23)\nv_mfma_f32_16x16x32_bf16 %[c2], a[64:67], v[196:199], %[c2]\n" \
  "v_mfma_f32_16x16x32_bf16 %[c3], a[68:71], v[196:199], %[c3]\nglobal_load_dwordx4 v[196:199], %[l16], s[90:91] offset:-3072 sc1\ns_waitcnt vmcnt(23)\nv_mfma_f32_16x16x32_bf16 %[c0], a[72:75], v[200:203], %[c0]\n" \
  "v_mfma_f32_16x16x32_bf16 %[c1], a[76:79], v[200:203], %[c1]\nglobal_load_dwordx4 v[200:203], %[l16], s[90:91] offset:-2048 sc1\ns_waitcnt vmcnt(23)\nv_mfma_f32_16x16x32_bf16 %[c2], a[72:75], v[204:207], %[c2]\n" \
  "v_mfma_f32_16x16x32_bf16 %[c3], a[76:79], v[204:207], %[c3]\nglobal_load_dwordx4 v[204:207], %[l16], s[90:91] offset:-1024 sc1\ns_waitcnt vmcnt(23)\nv_mfma_f32_16x16x32_bf16 %[c0], a[80:83], v[208:211], %[c0]\n" \
  "v_mfma_f32_16x16x32_bf16 %[c1], a[84:87], v[208:211], %[c1]\nglobal_load_dwordx4 v[208:211], %[l16], s[90:91] offset:0 sc1\ns_waitcnt vmcnt(23)\nv_mfma_f32_16x16x32_bf16 %[c2], a[80:83], v[212:215], %[c2]\n" \
  "v_mfma_f32_16x16x32_bf16 %[c3], a[84:87], v[212:215], %[c3]\nglobal_load_dwordx4 v[212:215], %[l16], s[90:91] offset:1024 sc1\ns_waitcnt vmcnt(23)\nv_mfma_f32_16x16x32_bf16 %[c0], a[88:91], v[216:219], %[c0]\n" \
  "v_mfma_f32_16x16x32_bf16 %[c1], a[92:95], v[216:219], %[c1]\nglobal_load_dwordx4 v[216:219], %[l16], s[90:91] offset:2048 sc1\ns_waitcnt vmcnt(23)\nv_mfma_f32_16x16x32_bf16 %[c2], a[88:91], v[220:223], %[c2]\n" \
  "v_mfma_f32_16x16x32_bf16 %[c3], a[92:95], v[220:223], %[c3]\nglobal_load_dwordx4 v[220:223], %[l16], s[90:91] offset:3072 sc1\ns_waitcnt vmcnt(23)\nv_mfma_f32_16x16x32_bf16 %[c0], a[96:99], v[128:131], %[c0]\n" \
  "v_mfma_f32_16x16x32_bf16 %[c1], a[100:103], v[128:131], %[c1]\ns_add_u32 s90, s90, 16384\ns_addc_u32 s91, s91, 0\nglobal_load_dwordx4 v[128:131], %[l16], s[90:91] offset:-4096 sc1\n" \
  "s_waitcnt vmcnt(23)\nv_mfma_f32_16x16x32_bf16 %[c2], a[96:99], v[132:135], %[c2]\nv_mfma_f32_16x16x32_bf16 %[c3], a[100:103], v[132:135], %[c3]\nglobal_load_dwordx4 v[132:135], %[l16], s[90:91] offset:-3072 sc1\n" \
  "s_waitcnt vmcnt(23)\nv_mfma_f32_16x16x32_bf16 %[c0], a[104:107], v[136:139], %[c0]\nv_mfma_f32_16x16x32_bf16 %[c1], a[108:111], v[136:139], %[c1]\nglobal_load_dwordx4 v[136:139], %[l16], s[90:91] offset:-2048 sc1\n" \
  "s_waitcnt vmcnt(23)\nv_mfma_f32_16x16x32_bf16 %[c2], a[104:107], v[140:143], %[c2]\nv_mfma_f32_16x16x32_bf16 %[c3], a[108:111], v[140:143], %[c3]\nglobal_load_dwordx4 v[140:143], %[l16], s[90:91] offset:-1024 sc1\n" \
  "s_waitcnt vmcnt(23)\nv_mfma_f32_16x16x32_bf16 %[c0], a[112:115], v[144:147], %[c0]\nv_mfma_f32_16x16x32_bf16 %[c1], a[116:119], v[144:147], %[c1]\nglobal_load_dwordx4 v[144:147], %[l16], s[90:91] offset:0 sc1\n" \
  "s_waitcnt vmcnt(23)\nv_mfma_f32_16x16x32_bf16 %[c2], a[112:115], v[148:151], %[c2]\nv_mfma_f32_16x16x32_bf16 %[c3], a[116:119], v[148:151], %[c3]\nglobal_load_dwordx4 v[148:151], %[l16], s[90:91] offset:1024 sc1\n" \
  "s_waitcnt vmcnt(23)\nv_mfma_f32_16x16x32_bf16 %[c0], a[120:123], v[152:155], %[c0]\nv_mfma_f32_16x16x32_bf16 %[c1], a[124:127], v[152:155], %[c1]\nglobal_load_dwordx4 v[152:155], %[l16], s[90:91] offset:2048 sc1\n" \
  "s_waitcnt vmcnt(23)\nv_mfma_f32_16x16x32_bf16 %[c2], a[120:123], v[156:159], %[c2]\nv_mfma_f32_16x16x32_bf16 %[c3], a[124:127], v[156:159], %[c3]\nglobal_load_dwordx4 v[156:159], %[l16], s[90:91] offset:3072 sc1\n" \
  "s_waitcnt vmcnt(23)\nv_mfma_f32_16x16x32_bf16 %[c0], a[128:131], v[160:163], %[c0]\nv_mfma_f32_16x16x32_bf16 %[c1], a[132:135], v[160:163], %[c1]\ns_add_u32 s90, s90, 16384\n" \
  "s_addc_u32 s91, s91, 0\nglobal_load_dwordx4 v[160:163], %[l16], s[90:91] offset:-4096 sc1\ns_waitcnt vmcnt(23)\nv_mfma_f32_16x16x32_bf16 %[c2], a[128:131], v[164:167], %[c2]\n" \
  "v_mfma_f32_16x16x32_bf16 %[c3], a[132:135], v[164:167], %[c3]\nglobal_load_dwordx4 v[164:167], %[l16], s[90:91] offset:-3072 sc1\ns_waitcnt vmcnt(23)\n" \
  "v_mfma_f32_16x16x32_bf16 %[c0], a[136:139], v[168:171], %[c0]\nv_mfma_f32_16x16x32_bf16 %[c1], a[140:143], v[168:171], %[c1]\nglobal_load_dwordx4 v[168:171], %[l16], s[90:91] offset:-2048 sc1\n" \
  "s_waitcnt vmcnt(23)\nv_mfma_f32_16x16x32_bf16 %[c2], a[136:139], v[172:175], %[c2]\nv_mfma_f32_16x16x32_bf16 %[c3], a[140:143], v[172:175], %[c3]\nglobal_load_dwordx4 v[172:175], %[l16], s[90:91] offset:-1024 sc1\n" \
  "s_waitcnt vmcnt(23)\nv_mfma_f32_16x16x32_bf16 %[c0], a[144:147], v[176:179], %[c0]\nv_mfma_f32_16x16x32_bf16 %[c1], a[148:151], v[176:179], %[c1]\nglobal_load_dwordx4 v[176:179], %[l16], s[90:91] offset:0 sc1\n" \
  "s_waitcnt vmcnt(23)\nv_mfma_f32_16x16x32_bf16 %[c2], a[144:147], v[180:183], %[c2]\nv_mfma_f32_16x16x32_bf16 %[c3], a[148:151], v[180:183], %[c3]\nglobal_load_dwordx4 v[180:183], %[l16], s[90:91] offset:1024 sc1\n" \
  "s_waitcnt vmcnt(23)\nv_mfma_f32_16x16x32_bf16 %[c0], a[152:155], v[184:187], %[c0]\nv_mfma_f32_16x16x32_bf16 %[c1], a[156:159], v[184:187], %[c1]\nglobal_load_dwordx4 v[184:187], %[l16], s[90:91] offset:2048 sc1\n" \
  "s_waitcnt vmcnt(23)\nv_mfma_f32_16x16x32_bf16 %[c2], a[152:155], v[188:191], %[c2]\nv_mfma_f32_16x16x32_bf16 %[c3], a[156:159], v[188:191], %[c3]\nglobal_load_dwordx4 v[188:191], %[l16], s[90:91] offset:3072 sc1\n" \
  "s_waitcnt vmcnt(23)\nv_mfma_f32_16x16x32_bf16 %[c0], a[160:163], v[192:195], %[c0]\nv_mfma_f32_16x16x32_bf16 %[c1], a[164:167], v[192:195], %[c1]\ns_waitcnt vmcnt(22)\n" \
  "v_mfma_f32_16x16x32_bf16 %[c2], a[160:163], v[196:199], %[c2]\nv_mfma_f32_16x16x32_bf16 %[c3], a[164:167], v[196:199], %[c3]\ns_waitcnt vmcnt(21)\nv_mfma_f32_16x16x32_bf16 %[c0], a[168:171], v[200:203], %[c0]\n" \
  "v_mfma_f32_16x16x32_bf16 %[c1], a[172:175], v[200:203], %[c1]\ns_waitcnt vmcnt(20)\nv_mfma_f32_16x16x32_bf16 %[c2], a[168:171], v[204:207], %[c2]\nv_mfma_f32_16x16x32_bf16 %[c3], a[172:175], v[204:207], %[c3]\n" \
  "s_waitcnt vmcnt(19)\nv_mfma_f32_16x16x32_bf16 %[c0], a[176:179], v[208:211], %[c0]\nv_mfma_f32_16x16x32_bf16 %[c1], a[180:183], v[208:211], %[c1]\ns_waitcnt vmcnt(18)\n" \
  "v_mfma_f32_16x16x32_bf16 %[c2], a[176:179], v[212:215], %[c2]\nv_mfma_f32_16x16x32_bf16 %[c3], a[180:183], v[212:215], %[c3]\ns_waitcnt vmcnt(17)\nv_mfma_f32_16x16x32_bf16 %[c0], a[184:187], v[216:219], %[c0]\n" \
  "v_mfma_f32_16x16x32_bf16 %[c1], a[188:191], v[216:219], %[c1]\ns_waitcnt vmcnt(16)\nv_mfma_f32_16x16x32_bf16 %[c2], a[184:187], v[220:223], %[c2]\nv_mfma_f32_16x16x32_bf16 %[c3], a[188:191], v[220:223], %[c3]\n" \
  "s_waitcnt vmcnt(15)\nv_mfma_f32_16x16x32_bf16 %[c0], a[192:195], v[128:131], %[c0]\nv_mfma_f32_16x16x32_bf16 %[c1], a[196:199], v[128:131], %[c1]\ns_waitcnt vmcnt(14)\n" \
  "v_mfma_f32_16x16x32_bf16 %[c2], a[192:195], v[132:135], %[c2]\nv_mfma_f32_16x16x32_bf16 %[c3], a[196:199], v[132:135], %[c3]\ns_waitcnt vmcnt(13)\nv_mfma_f32_16x16x32_bf16 %[c0], a[200:203], v[136:139], %[c0]\n" \
  "v_mfma_f32_16x16x32_bf16 %[c1], a[204:207], v[136:139], %[c1]\ns_waitcnt vmcnt(12)\nv_mfma_f32_16x16x32_bf16 %[c2], a[200:203], v[140:143], %[c2]\nv_mfma_f32_16x16x32_bf16 %[c3], a[204:207], v[140:143], %[c3]\n" \
  "s_waitcnt vmcnt(11)\nv_mfma_f32_16x16x32_bf16 %[c0], a[208:211], v[144:147], %[c0]\nv_mfma_f32_16x16x32_bf16 %[c1], a[212:215], v[144:147], %[c1]\ns_waitcnt vmcnt(10)\n" \
  "v_mfma_f32_16x16x32_bf16 %[c2], a[208:211], v[148:151], %[c2]\nv_mfma_f32_16x16x32_bf16 %[c3], a[212:215], v[148:151], %[c3]\ns_waitcnt vmcnt(9)\nv_mfma_f32_16x16x32_bf16 %[c0], a[216:219], v[152:155], %[c0]\n" \
  "v_mfma_f32_16x16x32_bf16 %[c1], a[220:223], v[152:155], %[c1]\ns_waitcnt vmcnt(8)\nv_mfma_f32_16x16x32_bf16 %[c2], a[216:219], v[156:159], %[c2]\nv_mfma_f32_16x16x32_bf16 %[c3], a[220:223], v[156:159], %[c3]\n" \
  "s_waitcnt vmcnt(7)\nv_mfma_f32_16x16x32_bf16 %[c0], a[224:227], v[160:163], %[c0]\nv_mfma_f32_16x16x32_bf16 %[c1], a[228:231], v[160:163], %[c1]\ns_waitcnt vmcnt(6)\n" \
  "v_mfma_f32_16x16x32_bf16 %[c2], a[224:227], v[164:167], %[c2]\nv_mfma_f32_16x16x32_bf16 %[c3], a[228:231], v[164:167], %[c3]\ns_waitcnt vmcnt(5)\nv_mfma_f32_16x16x32_bf16 %[c0], a[232:235], v[168:171], %[c0]\n" \
  "v_mfma_f32_16x16x32_bf16 %[c1], a[236:239], v[168:171], %[c1]\ns_waitcnt vmcnt(4)\nv_mfma_f32_16x16x32_bf16 %[c2], a[232:235], v[172:175], %[c2]\nv_mfma_f32_16x16x32_bf16 %[c3], a[236:239], v[172:175], %[c3]\n" \
  "s_waitcnt vmcnt(3)\nv_mfma_f32_16x16x32_bf16 %[c0], a[240:243], v[176:179], %[c0]\nv_mfma_f32_16x16x32_bf16 %[c1], a[244:247], v[176:179], %[c1]\ns_waitcnt vmcnt(2)\n" \
  "v_mfma_f32_16x16x32_bf16 %[c2], a[240:243], v[180:183], %[c2]\nv_mfma_f32_16x16x32_bf16 %[c3], a[244:247], v[180:183], %[c3]\ns_waitcnt vmcnt(1)\nv_mfma_f32_16x16x32_bf16 %[c0], a[248:251], v[184:187], %[c0]\n" \
  "v_mfma_f32_16x16x32_bf16 %[c1], a[252:255], v[184:187], %[c1]\ns_waitcnt vmcnt(0)\nv_mfma_f32_16x16x32_bf16 %[c2], a[248:251], v[188:191], %[c2]\nv_mfma_f32_16x16x32_bf16 %[c3], a[252:255], v[188:191], %[c3]\n" \
  "s_nop 7\n"
#define R3_MAIN_CLOB \
  "v128","v129","v130","v131","v132","v133","v134","v135","v136","v137","v138","v139","v140","v141","v142","v143","v144","v145","v146","v147","v148","v149", \
  "v150","v151","v152","v153","v154","v155","v156","v157","v158","v159","v160","v161","v162","v163","v164","v165","v166","v167","v168","v169","v170","v171", \
  "v172","v173","v174","v175","v176","v177","v178","v179","v180","v181","v182","v183","v184","v185","v186","v187","v188","v189","v190","v191","v192","v193", \
  "v194","v195","v196","v197","v198","v199","v200","v201","v202","v203","v204","v205","v206","v207","v208","v209","v210","v211","v212","v213","v214","v215", \
  "v216","v217","v218","v219","v220","v221","v222","v223","s90","s91","s92","s93"
#define R3_HALF_ASM \
  "s_mov_b64 s[90:91], %[src]\nglobal_load_dwordx4 v[120:123], %[l16], s[90:91] offset:-4096 sc1\nglobal_load_dwordx4 v[124:127], %[l16], s[90:91] offset:-2048 sc1\n" \
  "global_load_dwordx4 v[128:131], %[l16], s[90:91] offset:0 sc1\nglobal_load_dwordx4 v[132:135], %[l16], s[90:91] offset:2048 sc1\ns_add_u32 s90, s90, 16384\n" \
  "s_addc_u32 s91, s91, 0\nglobal_load_dwordx4 v[136:139], %[l16], s[90:91] offset:-4096 sc1\nglobal_load_dwordx4 v[140:143], %[l16], s[90:91] offset:-2048 sc1\n" \
  "global_load_dwordx4 v[144:147], %[l16], s[90:91] offset:0 sc1\nglobal_load_dwordx4 v[148:151], %[l16], s[90:91] offset:2048 sc1\ns_add_u32 s90, s90, 16384\n" \
  "s_addc_u32 s91, s91, 0\nglobal_load_dwordx4 v[152:155], %[l16], s[90:91] offset:-4096 sc1\nglobal_load_dwordx4 v[156:159], %[l16], s[90:91] offset:-2048 sc1\n" \
  "global_load_dwordx4 v[160:163], %[l16], s[90:91] offset:0 sc1\nglobal_load_dwordx4 v[164:167], %[l16], s[90:91] offset:2048 sc1\ns_add_u32 s90, s90, 16384\n" \
  "s_addc_u32 s91, s91, 0\nglobal_load_dwordx4 v[168:171], %[l16], s[90:91] offset:-4096 sc1\nglobal_load_dwordx4 v[172:175], %[l16], s[90:91] offset:-2048 sc1\n" \
  "global_load_dwordx4 v[176:179], %[l16], s[90:91] offset:0 sc1\nglobal_load_dwordx4 v[180:183], %[l16], s[90:91] offset:2048 sc1\ns_add_u32 s90, s90, 16384\n" \
  "s_addc_u32 s91, s91, 0\nglobal_load_dwordx4 v[184:187], %[l16], s[90:91] offset:-4096 sc1\nglobal_load_dwordx4 v[188:191], %[l16], s[90:91] offset:-2048 sc1\n" \
  "global_load_dwordx4 v[192:195], %[l16], s[90:91] offset:0 sc1\nglobal_load_dwordx4 v[196:199], %[l16], s[90:91] offset:2048 sc1\ns_add_u32 s90, s90, 16384\n" \
  "s_addc_u32 s91, s91, 0\nglobal_load_dwordx4 v[200:203], %[l16], s[90:91] offset:-4096 sc1\nglobal_load_dwordx4 v[204:207], %[l16], s[90:91] offset:-2048 sc1\n" \
  "global_load_dwordx4 v[208:211], %[l16], s[90:91] offset:0 sc1\nglobal_load_dwordx4 v[212:215], %[l16], s[90:91] offset:2048 sc1\ns_add_u32 s90, s90, 16384\n" \
  "s_addc_u32 s91, s91, 0\nglobal_load_dwordx4 v[216:219], %[l16], s[90:91] offset:-4096 sc1\nglobal_load_dwordx4 v[220:223], %[l16], s[90:91] offset:-2048 sc1\n" \
  "global_load_dwordx4 v[224:227], %[l16], s[90:91] offset:0 sc1\nglobal_load_dwordx4 v[228:231], %[l16], s[90:91] offset:2048 sc1\ns_add_u32 s90, s90, 16384\n" \
  "s_addc_u32 s91, s91, 0\nglobal_load_dwordx4 v[232:235], %[l16], s[90:91] offset:-4096 sc1\nglobal_load_dwordx4 v[236:239], %[l16], s[90:91] offset:-2048 sc1\n" \
  "global_load_dwordx4 v[240:243], %[l16], s[90:91] offset:0 sc1\nglobal_load_dwordx4 v[244:247], %[l16], s[90:91] offset:2048 sc1\ns_waitcnt vmcnt(31)\nv_mfma_f32_16x16x32_bf16 %[c0], a[0:3], v[120:123], 0\n" \
  "v_mfma_f32_16x16x32_bf16 %[c1], a[4:7], v[120:123], 0\ns_waitcnt vmcnt(30)\nv_mfma_f32_16x16x32_bf16 %[c0], a[8:11], v[124:127], %[c0]\nv_mfma_f32_16x16x32_bf16 %[c1], a[12:15], v[124:127], %[c1]\n" \
  "s_waitcnt vmcnt(29)\nv_mfma_f32_16x16x32_bf16 %[c0], a[16:19], v[128:131], %[c0]\nv_mfma_f32_16x16x32_bf16 %[c1], a[20:23], v[128:131], %[c1]\ns_waitcnt vmcnt(28)\n" \
  "v_mfma_f32_16x16x32_bf16 %[c0], a[24:27], v[132:135], %[c0]\nv_mfma_f32_16x16x32_bf16 %[c1], a[28:31], v[132:135], %[c1]\ns_waitcnt vmcnt(27)\nv_mfma_f32_16x16x32_bf16 %[c0], a[32:35], v[136:139], %[c0]\n" \
  "v_mfma_f32_16x16x32_bf16 %[c1], a[36:39], v[136:139], %[c1]\ns_waitcnt vmcnt(26)\nv_mfma_f32_16x16x32_bf16 %[c0], a[40:43], v[140:143], %[c0]\nv_mfma_f32_16x16x32_bf16 %[c1], a[44:47], v[140:143], %[c1]\n" \
  "s_waitcnt vmcnt(25)\nv_mfma_f32_16x16x32_bf16 %[c0], a[48:51], v[144:147], %[c0]\nv_mfma_f32_16x16x32_bf16 %[c1], a[52:55], v[144:147], %[c1]\ns_waitcnt vmcnt(24)\n" \
  "v_mfma_f32_16x16x32_bf16 %[c0], a[56:59], v[148:151], %[c0]\nv_mfma_f32_16x16x32_bf16 %[c1], a[60:63], v[148:151], %[c1]\ns_waitcnt vmcnt(23)\nv_mfma_f32_16x16x32_bf16 %[c0], a[64:67], v[152:155], %[c0]\n" \
  "v_mfma_f32_16x16x32_bf16 %[c1], a[68:71], v[152:155], %[c1]\ns_waitcnt vmcnt(22)\nv_mfma_f32_16x16x32_bf16 %[c0], a[72:75], v[156:159], %[c0]\nv_mfma_f32_16x16x32_bf16 %[c1], a[76:79], v[156:159], %[c1]\n" \
  "s_waitcnt vmcnt(21)\nv_mfma_f32_16x16x32_bf16 %[c0], a[80:83], v[160:163], %[c0]\nv_mfma_f32_16x16x32_bf16 %[c1], a[84:87], v[160:163], %[c1]\ns_waitcnt vmcnt(20)\n" \
  "v_mfma_f32_16x16x32_bf16 %[c0], a[88:91], v[164:167], %[c0]\nv_mfma_f32_16x16x32_bf16 %[c1], a[92:95], v[164:167], %[c1]\ns_waitcnt vmcnt(19)\nv_mfma_f32_16x16x32_bf16 %[c0], a[96:99], v[168:171], %[c0]\n" \
  "v_mfma_f32_16x16x32_bf16 %[c1], a[100:103], v[168:171], %[c1]\ns_waitcnt vmcnt(18)\nv_mfma_f32_16x16x32_bf16 %[c0], a[104:107], v[172:175], %[c0]\nv_mfma_f32_16x16x32_bf16 %[c1], a[108:111], v[172:175], %[c1]\n" \
  "s_waitcnt vmcnt(17)\nv_mfma_f32_16x16x32_bf16 %[c0], a[112:115], v[176:179], %[c0]\nv_mfma_f32_16x16x32_bf16 %[c1], a[116:119], v[176:179], %[c1]\ns_waitcnt vmcnt(16)\n" \
  "v_mfma_f32_16x16x32_bf16 %[c0], a[120:123], v[180:183], %[c0]\nv_mfma_f32_16x16x32_bf16 %[c1], a[124:127], v[180:183], %[c1]\ns_waitcnt vmcnt(15)\nv_mfma_f32_16x16x32_bf16 %[c0], a[128:131], v[184:187], %[c0]\n" \
  "v_mfma_f32_16x16x32_bf16 %[c1], a[132:135], v[184:187], %[c1]\ns_waitcnt vmcnt(14)\nv_mfma_f32_16x16x32_bf16 %[c0], a[136:139], v[188:191], %[c0]\nv_mfma_f32_16x16x32_bf16 %[c1], a[140:143], v[188:191], %[c1]\n" \
  "s_waitcnt vmcnt(13)\nv_mfma_f32_16x16x32_bf16 %[c0], a[144:147], v[192:195], %[c0]\nv_mfma_f32_16x16x32_bf16 %[c1], a[148:151], v[192:195], %[c1]\ns_waitcnt vmcnt(12)\n" \
  "v_mfma_f32_16x16x32_bf16 %[c0], a[152:155], v[196:199], %[c0]\nv_mfma_f32_16x16x32_bf16 %[c1], a[156:159], v[196:199], %[c1]\ns_waitcnt vmcnt(11)\nv_mfma_f32_16x16x32_bf16 %[c0], a[160:163], v[200:203], %[c0]\n" \
  "v_mfma_f32_16x16x32_bf16 %[c1], a[164:167], v[200:203], %[c1]\ns_waitcnt vmcnt(10)\nv_mfma_f32_16x16x32_bf16 %[c0], a[168:171], v[204:207], %[c0]\nv_mfma_f32_16x16x32_bf16 %[c1], a[172:175], v[204:207], %[c1]\n" \
  "s_waitcnt vmcnt(9)\nv_mfma_f32_16x16x32_bf16 %[c0], a[176:179], v[208:211], %[c0]\nv_mfma_f32_16x16x32_bf16 %[c1], a[180:183], v[208:211], %[c1]\ns_waitcnt vmcnt(8)\n" \
  "v_mfma_f32_16x16x32_bf16 %[c0], a[184:187], v[212:215], %[c0]\nv_mfma_f32_16x16x32_bf16 %[c1], a[188:191], v[212:215], %[c1]\ns_waitcnt vmcnt(7)\nv_mfma_f32_16x16x32_bf16 %[c0], a[192:195], v[216:219], %[c0]\n" \
  "v_mfma_f32_16x16x32_bf16 %[c1], a[196:199], v[216:219], %[c1]\ns_waitcnt vmcnt(6)\nv_mfma_f32_16x16x32_bf16 %[c0], a[200:203], v[220:223], %[c0]\nv_mfma_f32_16x16x32_bf16 %[c1], a[204:207], v[220:223], %[c1]\n" \
  "s_waitcnt vmcnt(5)\nv_mfma_f32_16x16x32_bf16 %[c0], a[208:211], v[224:227], %[c0]\nv_mfma_f32_16x16x32_bf16 %[c1], a[212:215], v[224:227], %[c1]\ns_waitcnt vmcnt(4)\n" \
  "v_mfma_f32_16x16x32_bf16 %[c0], a[216:219], v[228:231], %[c0]\nv_mfma_f32_16x16x32_bf16 %[c1], a[220:223], v[228:231], %[c1]\ns_waitcnt vmcnt(3)\nv_mfma_f32_16x16x32_bf16 %[c0], a[224:227], v[232:235], %[c0]\n" \
  "v_mfma_f32_16x16x32_bf16 %[c1], a[228:231], v[232:235], %[c1]\ns_waitcnt vmcnt(2)\nv_mfma_f32_16x16x32_bf16 %[c0], a[232:235], v[236:239], %[c0]\nv_mfma_f32_16x16x32_bf16 %[c1], a[236:239], v[236:239], %[c1]\n" \
  "s_waitcnt vmcnt(1)\nv_mfma_f32_16x16x32_bf16 %[c0], a[240:243], v[240:243], %[c0]\nv_mfma_f32_16x16x32_bf16 %[c1], a[244:247], v[240:243], %[c1]\ns_waitcnt vmcnt(0)\n" \
  "v_mfma_f32_16x16x32_bf16 %[c0], a[248:251], v[244:247], %[c0]\nv_mfma_f32_16x16x32_bf16 %[c1], a[252:255], v[244:247], %[c1]\ns_nop 7\n"
#define R3_HALF_CLOB \
  "v120","v121","v122","v123","v124","v125","v126","v127","v128","v129","v130","v131","v132","v133","v134","v135","v136","v137","v138","v139","v140","v141", \
  "v142","v143","v144","v145","v146","v147","v148","v149","v150","v151","v152","v153","v154","v155","v156","v157","v158","v159","v160","v161","v162","v163", \
  "v164","v165","v166","v167","v168","v169","v170","v171","v172","v173","v174","v175","v176","v177","v178","v179","v180","v181","v182","v183","v184","v185", \
  "v186","v187","v188","v189","v190","v191","v192","v193","v194","v195","v196","v197","v198","v199","v200","v201","v202","v203","v204","v205","v206","v207", \
  "v208","v209","v210","v211","v212","v213","v214","v215","v216","v217","v218","v219","v220","v221","v222","v223","v224","v225","v226","v227","v228","v229", \
  "v230","v231","v232","v233","v234","v235","v236","v237","v238","v239","v240","v241","v242","v243","v244","v245","v246","v247","s90","s91"

typedef unsigned u32x4 __attribute__((ext_vector_type(4)));
typedef unsigned u32x2 __attribute__((ext_vector_type(2)));
typedef unsigned u32x32 __attribute__((ext_vector_type(32)));
typedef unsigned u32x16 __attribute__((ext_vector_type(16)));
#define WREG_DECL u32x32 wa0, wa1, wa2, wa3, wa4, wa5, wa6, wa7;
#define WREG_OUT7 "={a[0:31]}"(wa0), "={a[32:63]}"(wa1), "={a[64:95]}"(wa2), "={a[96:127]}"(wa3), "={a[128:159]}"(wa4), "={a[160:191]}"(wa5), "={a[192:223]}"(wa6)
#define WREG_IN7 "{a[0:31]}"(wa0), "{a[32:63]}"(wa1), "{a[64:95]}"(wa2), "{a[96:127]}"(wa3), "{a[128:159]}"(wa4), "{a[160:191]}"(wa5), "{a[192:223]}"(wa6)
#define WREG_OUT WREG_OUT7, "={a[224:255]}"(wa7)
#define WREG_IN WREG_IN7, "{a[224:255]}"(wa7)
typedef float f32x4 __attribute__((ext_vector_type(4)));
typedef float f32x2 __attribute__((ext_vector_type(2)));
typedef __bf16 bf16x2 __attribute__((ext_vector_type(2)));
typedef __bf16 bf16x8 __attribute__((ext_vector_type(8)));
#define GAS __attribute__((address_space(1)))
#define RLX_AGENT __ATOMIC_RELAXED, __HIP_MEMORY_SCOPE_AGENT
#define DI __device__ __forceinline__

constexpr int LDS_BYTES = 163840;
constexpr int NCHAIN = 128;
#define TOUCH_FROM 1
#define TOUCH_DELAY
#define FAR_BIT 0x2000u
#define T_WORK 152
#define T_EARLY 12
#define T_RED 100
constexpr size_t OFF_FLAGS = 4u << 20;
constexpr size_t OFF_BOOT = OFF_FLAGS + (256u << 10);
constexpr size_t OFF_P2 = (6u << 20) + 8192;
constexpr size_t OFF_DBG = 232u << 20;
constexpr unsigned L_XN = 0;
constexpr unsigned L_KX = L_XN + (4u << 20);
constexpr unsigned L_VX = L_KX + (17u << 20);
constexpr unsigned L_KM = L_VX + (17u << 20);
constexpr unsigned L_VM = L_KM + (8u << 20);
constexpr unsigned L_H = L_VM + (4u << 20);
constexpr unsigned L_O = L_H + (8u << 20);
constexpr unsigned L_G = L_O + (16u << 20);
constexpr unsigned L_QX = L_G + (4u << 20);
__host__ __device__ constexpr unsigned far_even(unsigned o) { return (8u << 20) + ((o >> 13) << 14) + (o & 8191u); }
__host__ __device__ constexpr unsigned far_off(unsigned o) { return far_even(o) | ((~far_even(o) >> 8) & FAR_BIT); }
__host__ __device__ constexpr unsigned near_off(unsigned o) { return far_off(o) ^ FAR_BIT; }
constexpr int FL_ERR = 0, FL_RDY = 600, FL_PK = 8, FL_PV = 72, FL_PH = 136, FL_PG = 680, FL_UC = 664, FL_NUL = 1, FL_PO = 184  , FL_PQ = 1024, FL_PM = 320, FL_H1 = 760, FL_H2 = 888;

struct Params {
  const uint16_t *x, *mem0, *wq, *wk, *wv, *wo, *w1, *b1, *w2;
  uint16_t* out;
  unsigned char* ws;
  int tw, tr;
};

DI unsigned rfl(unsigned v) { return __builtin_amdgcn_readfirstlane(v); }
DI unsigned su(unsigned v) { unsigned r; asm("s_nop 0\n\tv_readfirstlane_b32 %0, %1\n\ts_nop 4" : "=s"(r) : "v"(v)); return r; }
DI unsigned pk2(float lo, float hi) { f32x2 v = {lo, hi}; return __builtin_bit_cast(unsigned, __builtin_convertvector(v, bf16x2)); }
DI float bflo(unsigned u) { return __builtin_bit_cast(float, u << 16); }
DI float bfhi(unsigned u) { return __builtin_bit_cast(float, u & 0xffff0000u); }
DI float rbf(float v) { return bflo(pk2(v, v) & 0xffffu); }
DI __amdgpu_buffer_rsrc_t rsrc(const void* p, unsigned bytes) {
  const uint64_t a = (uint64_t)p;
  return __builtin_amdgcn_make_buffer_rsrc((void*)(((uint64_t)rfl((unsigned)(a >> 32)) << 32) | rfl((unsigned)a)), 0, bytes, 0x00020000);
}
DI void drain() { asm volatile("s_waitcnt vmcnt(0)" ::: "memory"); }
DI void wg_barrier() { asm volatile("s_waitcnt lgkmcnt(0)\n\ts_barrier" ::: "memory"); }
DI void publish(unsigned* flags, int line, unsigned v, int lane) {
  if (lane == 0) __hip_atomic_store((GAS unsigned*)(flags + line * 32), v, RLX_AGENT);
}
#define WACC
#define WARG
DI bool wait_ge(unsigned* flags, int line, unsigned need, unsigned& last WACC) {
  if (__all(last >= need)) return true;
  for (unsigned spin = 0; spin < (1u << 21); ++spin) {
    const unsigned v = __hip_atomic_load((GAS unsigned*)(flags + line * 32), RLX_AGENT);
    if (__all(v >= need)) { last = v; asm volatile("" ::: "memory"); return true; }
    __builtin_amdgcn_s_sleep(4);
  }
  __hip_atomic_store((GAS unsigned*)(flags + FL_ERR * 32), 3u, RLX_AGENT);
  return false;
}

constexpr unsigned BOOT_BAD = 1u;
struct Boot { const unsigned char *words, *mine, *epoch; unsigned long long salt; unsigned* flags; };
struct BootView { unsigned long long v0, v1, v2, v3; };
DI BootView boot_look(const Boot& bt, int l) {
  GAS unsigned long long* a = (GAS unsigned long long*)(bt.words + l * 128);
  return {__hip_atomic_load(a, RLX_AGENT), __hip_atomic_load(a + 1024, RLX_AGENT), __hip_atomic_load(a + 2048, RLX_AGENT), __hip_atomic_load(a + 3072, RLX_AGENT)};
}
DI unsigned wait_boot(const Boot& bt, int l, BootView v) {
  for (unsigned spin = 0; spin < (1u << 21); ++spin) {
    v.v0 ^= bt.salt; v.v1 ^= bt.salt; v.v2 ^= bt.salt; v.v3 ^= bt.salt;
    if (__all((v.v0 | v.v1 | v.v2 | v.v3) < 32)) break;
    __builtin_amdgcn_s_sleep(2);
    v = boot_look(bt, l);
  }
  __builtin_amdgcn_s_waitcnt(0x0F70);
  asm volatile("" ::: "memory");
  const int votes = __popcll(__ballot(v.v0 & 1)) + __popcll(__ballot(v.v1 & 1)) + __popcll(__ballot(v.v2 & 1)) + __popcll(__ballot(v.v3 & 1));
  unsigned r = (votes > 28 ? 0x2000u : 0u) | (__all((v.v0 | v.v1 | v.v2 | v.v3) < 4) ? 0u : BOOT_BAD);
  asm volatile("s_nop 0\n\tv_readfirstlane_b32 %0, %1\n\ts_nop 3" : "=s"(r) : "v"(r));
  return r;
}

#define LOOPSTART
DI int dsel(int ks, int ch, int u, int jj) {
  if (ks < 8) return 160 * (ks >> 1) + 16 * (4 * (ks & 1) + (jj & 3)) + 8 * ch + 2 * u + (jj >> 2);
  return 160 * (2 * (ks - 8) + ch) + 16 * (8 + (jj & 1)) + 4 * u + (jj >> 1);
}

DI void layernorm_rows(const Params& p, int row0, int l) {
  const int seg = row0 >> 5, r = (row0 & 31) + (l & 7), i = l >> 3;
  u32x4 v[10];
#pragma unroll
  for (int j = 0; j < 10; ++j) v[j] = *(const u32x4*)(p.x + (size_t)(seg * 32 + r) * 640 + (i + 8 * j) * 8);
  float s = 0.f;
#pragma unroll
  for (int j = 0; j < 10; ++j)
#pragma unroll
    for (int k = 0; k < 4; ++k) s += bflo(v[j][k]) + bfhi(v[j][k]);
#pragma unroll
  for (int m = 8; m < 64; m <<= 1) s += __shfl_xor(s, m);
  const float mean = s * (1.f / 640.f);
  float q = 0.f;
#pragma unroll
  for (int j = 0; j < 10; ++j)
#pragma unroll
    for (int k = 0; k < 4; ++k) { const float d0 = bflo(v[j][k]) - mean, d1 = bfhi(v[j][k]) - mean; q += d0 * d0 + d1 * d1; }
#pragma unroll
  for (int m = 8; m < 64; m <<= 1) q += __shfl_xor(q, m);
  const float mb = rbf(mean), rs = __builtin_amdgcn_rcpf(rbf(__builtin_amdgcn_sqrtf(rbf(rbf(q * (1.f / 640.f)) + 1e-5f))));
  const __amdgpu_buffer_rsrc_t rx = rsrc(p.ws, 16u << 20);
  const unsigned dst = L_XN + seg * 65536 + (r >> 4) * 1024 + (r & 15) * 16;
#pragma unroll
  for (int j = 0; j < 10; ++j) {
    u32x4 o;
#pragma unroll
    for (int k = 0; k < 4; ++k) { const unsigned d = pk2(bflo(v[j][k]) - mb, bfhi(v[j][k]) - mb); o[k] = pk2(bflo(d) * rs, bfhi(d) * rs); }
    const int c = i + 8 * j;
    __builtin_amdgcn_raw_buffer_store_b128(o, rx, far_off(dst + (c >> 2) * 2048 + (c & 3) * 256), 0, 16);
  }
}

DI void init_m0(const Params& p, int r, int slot) {
  const int ks = r >> 3, g = (r >> 1) & 3, ch = r & 1, qp = (slot >> 1) & 1, u = (((slot >> 2) ^ (g >> 1)) << 1) | (slot & 1);
  const uint16_t* m = p.mem0 + (2 * g + qp) * 640;
  u32x4 v;
#pragma unroll
  for (int j = 0; j < 4; ++j) v[j] = min((unsigned)m[dsel(ks, ch, u, 2 * j)] | ((unsigned)m[dsel(ks, ch, u, 2 * j + 1)] << 16), 0xFFFFFFFEu);
  __builtin_amdgcn_raw_buffer_store_b128(v, rsrc(p.ws, 4u << 20), (r < 64 ? (2 << 20) + 8192 + r * 128 : 100 * 16384 + (r - 64) * 128) + slot * 16, 0, 16);
}
DI void init_line(const Params& p, int r, unsigned px, int l) {
  const __amdgpu_buffer_rsrc_t rw = rsrc(p.ws, 4u << 20);
#pragma unroll
  for (int k = 0; k < 8; ++k) {
    const int s = (l >> 3) + 8 * k;
    const int off = r < 64 ? (2 << 20) + 8192 + s * 16384 + r * 128 : (80 + (s >> 2)) * 16384 + (s & 3) * 2048 + (r - 64) * 128;
    if (s) __builtin_amdgcn_raw_buffer_store_b128((u32x4){~0u, ~0u, ~0u, ~0u}, rw, (off ^ px) + (l & 7) * 16, 0, 16);
  }
}

DI float mish(float h) {
  const float e = __builtin_amdgcn_exp2f(fabsf(h) * -1.44269504089f);
  const float a = h > 0.f ? 1.f : e, b2 = h > 0.f ? e + e : 2.f;
  const float n = a * (a + b2), d = __builtin_fmaf(0.5f * b2, b2, n);
  return h * n * __builtin_amdgcn_rcpf(d);
}

enum { KIND_K = 0, KIND_V = 1, KIND_F = 2 };

template <int KIND>
DI unsigned role_r1(const Params& p, unsigned char* lds, int idx, int grp, int helper, bool isq, Boot& bt, unsigned px, int w, int l) {
  unsigned* flags = bt.flags;
  const bool kv = KIND <= KIND_V && !isq;
  const int g = l >> 4, q = (l >> 2) & 3, pp = l & 3;
  const unsigned rs = KIND == KIND_F ? 3456u : 8192u;
  const uint16_t* wsrc = (KIND == KIND_K ? (isq ? p.wq : p.wk) : KIND == KIND_V ? p.wv : p.w1) + idx * 256;
  const unsigned bl = 81920u + w * 16384u + l * 16u;
#define XN_OPS(S, B) [src] "s"(p.ws + su(far_off(L_XN + (S) * 65536 + w * 8192))), [dst] "s"((B) * 40960 + w * 8192), \
                     [src2] "s"(p.ws + su(far_off(L_XN + (S) * 65536 + 32768 + w * 2048))), [dst2] "s"((B) * 40960 + 32768 + w * 2048)
  const int myline = (KIND == KIND_K ? (isq ? FL_PQ : FL_PK) : KIND == KIND_V ? FL_PV : FL_PH + grp * 584) + idx * 4 + w +
                     (helper ? (grp == 1 ? FL_H1 : FL_H2) : 0);
  WREG_DECL
  {
    const unsigned f = q | ((g & 1) << 2), tb = (8 * g + q) * 512 + pp * 8;
    const unsigned lim = (KIND == KIND_F && idx == 6) ? 368u : 496u;
    const int sw = (l & 2) ? myline / 17 : (l & 1) ? ((kv && grp == 0) ? 3 : grp + ((KIND == KIND_F || helper) ? 2 : 1)) : grp;
    unsigned slo = (unsigned)bt.salt, shi = (unsigned)(bt.salt >> 32);
    asm volatile(R1_LOAD_ASM
                 : WREG_OUT, [slo] "+s"(slo), [shi] "+s"(shi)
                 : [w] "s"(wsrc), [rs] "s"(rs), [wave] "s"(w),
                   [vc] "v"(((l & 31) * 16) ^ ((l >> 5) << 5)), [vrow] "v"((16 * w + (l >> 5)) * rs), [lim] "v"(lim),
                   [t0] "v"(tb + ((4 * w) ^ f) * 32), [bl] "v"(bl), XN_OPS(grp, 0), [l16] "v"(l * 16),
                   [ab] "s"(bt.words), [aw] "v"(((((sw & 3) - (int)(slo >> 2)) & 7) + 8 * (16 + (sw >> 2))) * 128),
                   [me] "s"(bt.mine), [ep] "s"(bt.epoch), [px] "s"(px)
                 : "memory", "scc", "vcc", R1_LOAD_CLOB);
    bt.salt = ((unsigned long long)shi << 32) | slo;
  }
  unsigned pub = 0;
  const int STEP = (KIND == KIND_F || helper) ? 2 : 1;
  float bias[16];
  if (KIND == KIND_F) {
#pragma unroll
    for (int k = 0; k < 16; ++k) { const int f = idx * 256 + w * 64 + (k >> 2) * 16 + g * 4 + (k & 3); bias[k] = f < 1728 ? bflo(p.b1[f]) : 0.f; }
  }
  const int hh = 2 * idx + (w >> 1);
  int send = helper ? 3 : 64;
  asm volatile("" : "+s"(send));
  int buf = 0;
  LOOPSTART
  for (int s = grp; s < send; buf ^= 1) {
    const int adv = (kv && s == 0) ? 3 : STEP;
    f32x4 c0, c1, c2, c3, c4, c5, c6, c7;
    const int sn = s + adv < 64 ? s + adv : s;
#define R1_OPS : [c0] "=&v"(c0), [c1] "=&v"(c1), [c2] "=&v"(c2), [c3] "=&v"(c3), [c4] "=&v"(c4), [c5] "=&v"(c5), [c6] "=&v"(c6), [c7] "=&v"(c7) \
               : [al] "v"(buf * 40960 + l * 16), [bl] "v"(bl), [l16] "v"(l * 16), XN_OPS(sn, buf ^ 1), [fl] "s"(flags + myline * 32), [fv] "s"(s < 4 ? pub : s), WREG_IN \
               : "memory", "scc", R1_MAIN_CLOB
    if (KIND == KIND_V) asm volatile(R1_MAIN_X_ASM R1_OPS);
    else asm volatile(R1_MAIN_W_ASM R1_OPS);
    if (KIND == KIND_F) {
      f32x4* cc[8] = {&c0, &c1, &c2, &c3, &c4, &c5, &c6, &c7};
#pragma unroll
      for (int t = 0; t < 8; ++t)
#pragma unroll
        for (int r = 0; r < 4; r += 2) {
          const unsigned u = pk2((*cc[t])[r], (*cc[t])[r + 1]);
          const unsigned hb = pk2(bflo(u) + bias[(t & 3) * 4 + r], bfhi(u) + bias[(t & 3) * 4 + r + 1]);
          (*cc[t])[r] = mish(bflo(hb)); (*cc[t])[r + 1] = mish(bfhi(hb));
        }
    }
    u32x4 fr[4];
    if (KIND == KIND_V) {
      fr[0] = (u32x4){pk2(c0[0], c0[1]), pk2(c0[2], c0[3]), pk2(c4[0], c4[1]), pk2(c4[2], c4[3])};
      fr[1] = (u32x4){pk2(c1[0], c1[1]), pk2(c1[2], c1[3]), pk2(c5[0], c5[1]), pk2(c5[2], c5[3])};
      fr[2] = (u32x4){pk2(c2[0], c2[1]), pk2(c2[2], c2[3]), pk2(c6[0], c6[1]), pk2(c6[2], c6[3])};
      fr[3] = (u32x4){pk2(c3[0], c3[1]), pk2(c3[2], c3[3]), pk2(c7[0], c7[1]), pk2(c7[2], c7[3])};
    } else {
      fr[0] = (u32x4){pk2(c0[0], c0[1]), pk2(c0[2], c0[3]), pk2(c1[0], c1[1]), pk2(c1[2], c1[3])};
      fr[1] = (u32x4){pk2(c4[0], c4[1]), pk2(c4[2], c4[3]), pk2(c5[0], c5[1]), pk2(c5[2], c5[3])};
      fr[2] = (u32x4){pk2(c2[0], c2[1]), pk2(c2[2], c2[3]), pk2(c3[0], c3[1]), pk2(c3[2], c3[3])};
      fr[3] = (u32x4){pk2(c6[0], c6[1]), pk2(c6[2], c6[3]), pk2(c7[0], c7[1]), pk2(c7[2], c7[3])};
    }
    const bool early = kv && s < 3;
    if (px == ~0u && !early) {
      px = wait_boot(bt, l, boot_look(bt, l));
      if (px & BOOT_BAD) { drain(); wg_barrier(); return px; }
    }
    drain();
    const unsigned char* dst = p.ws + ((early ? 0u : px) ^ far_off(KIND == KIND_F ? L_H + s * 131072 + ((idx * 4 + w) / 7) * 32768 + ((idx * 4 + w) % 7) * 4096
                                                             : (KIND == KIND_K ? (isq ? L_QX : L_KX) : L_VX) + (s * 32 + hh) * 8192 + (w & 1) * 4096));
    const __amdgpu_buffer_rsrc_t r = rsrc(dst, 4096);
    const unsigned keep = (KIND == KIND_F && idx == 6 && w == 3) ? 0u : ~0u;
#pragma unroll
    for (int k = 0; k < 4; ++k) __builtin_amdgcn_raw_buffer_store_b128(KIND == KIND_F ? fr[k] & keep : fr[k], r, l * 16 + k * 1024, 0, 16);
    if (s < 4 || s + STEP >= 64) { drain(); pub = helper ? 1 : s + 1; publish(flags, myline, pub, l); }
    wg_barrier();
    s += adv;
  }
  if (px == ~0u) px = wait_boot(bt, l, boot_look(bt, l));
  return px;
}

DI void role_at(const Params& p, int idx, int grp, unsigned* flags, unsigned px, int w, int l WACC) {
  const int hh = idx * 4 + w, pl = (hh >> 1) * 4 + 2 * (hh & 1) + (l & 1), myline = FL_PO + grp * 64 + hh;
  f32x4 cm;
#pragma unroll
  for (int r = 0; r < 4; ++r) cm[r] = (4 * (l >> 4) + r > (l & 15)) ? -1e30f : 0.f;
  unsigned lastA = 0, lastB = 0;
  LOOPSTART
  for (int s = grp; s < 64; s += 2) {
    const bool hs = (s == 1 || s == 2) && (l & 4) == 0;
    wait_ge(flags, ((l & 4) ? FL_PQ : (l & 2) ? FL_PV : FL_PK) + pl + (hs ? (s == 1 ? FL_H1 : FL_H2) : 0), hs ? 1 : s + 1, lastA WARG);
    if (s < 3) lastA = 0;
    const __amdgpu_buffer_rsrc_t rq = rsrc(p.ws + (px ^ far_off(L_QX + (s * 32 + hh) * 8192)), 8192);
    const __amdgpu_buffer_rsrc_t rk = rsrc(p.ws + ((s < 3 ? 0u : px) ^ far_off(L_KX + (s * 32 + hh) * 8192)), 8192);
    const __amdgpu_buffer_rsrc_t rv = rsrc(p.ws + ((s < 3 ? 0u : px) ^ far_off(L_VX + (s * 32 + hh) * 8192)), 8192);
    u32x4 qf[8], kx[8], vx[8];
#pragma unroll
    for (int k = 0; k < 8; ++k) qf[k] = __builtin_amdgcn_raw_buffer_load_b128(rq, l * 16 + k * 1024, 0, 16);
#pragma unroll
    for (int k = 0; k < 8; ++k) kx[k] = __builtin_amdgcn_raw_buffer_load_b128(rk, l * 16 + k * 1024, 0, 16);
#pragma unroll
    for (int k = 0; k < 8; ++k) vx[k] = __builtin_amdgcn_raw_buffer_load_b128(rv, l * 16 + k * 1024, 0, 16);
    f32x4 sa, sb0, sb1;
    asm volatile("s_nop 1\n\t"
                 "v_mfma_f32_16x16x32_bf16 %0, %3, %11, %19\n\tv_mfma_f32_16x16x32_bf16 %1, %3, %12, 0\n\tv_mfma_f32_16x16x32_bf16 %2, %4, %12, %19\n\t"
                 "v_mfma_f32_16x16x32_bf16 %0, %5, %13, %0\n\tv_mfma_f32_16x16x32_bf16 %1, %5, %14, %1\n\tv_mfma_f32_16x16x32_bf16 %2, %6, %14, %2\n\t"
                 "v_mfma_f32_16x16x32_bf16 %0, %7, %15, %0\n\tv_mfma_f32_16x16x32_bf16 %1, %7, %16, %1\n\tv_mfma_f32_16x16x32_bf16 %2, %8, %16, %2\n\t"
                 "v_mfma_f32_16x16x32_bf16 %0, %9, %17, %0\n\tv_mfma_f32_16x16x32_bf16 %1, %9, %18, %1\n\tv_mfma_f32_16x16x32_bf16 %2, %10, %18, %2\n\ts_nop 7"
                 : "=&v"(sa), "=&v"(sb0), "=&v"(sb1)
                 : "v"(kx[0]), "v"(kx[1]), "v"(kx[2]), "v"(kx[3]), "v"(kx[4]), "v"(kx[5]), "v"(kx[6]), "v"(kx[7]),
                   "v"(qf[0]), "v"(qf[1]), "v"(qf[2]), "v"(qf[3]), "v"(qf[4]), "v"(qf[5]), "v"(qf[6]), "v"(qf[7]), "v"(cm));
    wait_ge(flags, FL_PM + hh * 8 + (l & 7), s + 1, lastB WARG);
    const __amdgpu_buffer_rsrc_t rm = rsrc(p.ws + (px ^ near_off(L_KM + (s * 32 + hh) * 4096)), 4096);
    const __amdgpu_buffer_rsrc_t rn = rsrc(p.ws + (px ^ near_off(L_VM + (s * 32 + hh) * 2048)), 2048);
    u32x4 km[4]; u32x2 vm[8];
#pragma unroll
    for (int k = 0; k < 4; ++k) {
      const u32x2 lo = __builtin_amdgcn_raw_buffer_load_b64(rm, l * 8 + k * 1024, 0, 16), hi = __builtin_amdgcn_raw_buffer_load_b64(rm, l * 8 + k * 1024 + 512, 0, 16);
      km[k] = (u32x4){lo[0], lo[1], hi[0], hi[1]};
    }
#pragma unroll
    for (int k = 0; k < 8; ++k) vm[k] = (u32x2){__builtin_amdgcn_raw_buffer_load_b32(rn, l * 4 + k * 256, 0, 16), 0u};
    f32x4 m0, m1;
    asm volatile("s_nop 1\n\t"
                 "v_mfma_f32_16x16x32_bf16 %0, %2, %6, 0\n\tv_mfma_f32_16x16x32_bf16 %1, %2, %7, 0\n\t"
                 "v_mfma_f32_16x16x32_bf16 %0, %3, %8, %0\n\tv_mfma_f32_16x16x32_bf16 %1, %3, %9, %1\n\t"
                 "v_mfma_f32_16x16x32_bf16 %0, %4, %10, %0\n\tv_mfma_f32_16x16x32_bf16 %1, %4, %11, %1\n\t"
                 "v_mfma_f32_16x16x32_bf16 %0, %5, %12, %0\n\tv_mfma_f32_16x16x32_bf16 %1, %5, %13, %1\n\ts_nop 7"
                 : "=&v"(m0), "=&v"(m1)
                 : "v"(km[0]), "v"(km[1]), "v"(km[2]), "v"(km[3]), "v"(qf[0]), "v"(qf[1]), "v"(qf[2]), "v"(qf[3]), "v"(qf[4]), "v"(qf[5]), "v"(qf[6]), "v"(qf[7]));
    float mx0 = fmaxf(fmaxf(m0[0], m0[1]), fmaxf(fmaxf(sa[0], sa[1]), fmaxf(sa[2], sa[3])));
    float mx1 = fmaxf(fmaxf(fmaxf(m1[0], m1[1]), fmaxf(fmaxf(sb0[0], sb0[1]), fmaxf(sb0[2], sb0[3]))), fmaxf(fmaxf(sb1[0], sb1[1]), fmaxf(sb1[2], sb1[3])));
    mx0 = fmaxf(mx0, __shfl_xor(mx0, 16)); mx1 = fmaxf(mx1, __shfl_xor(mx1, 16));
    mx0 = fmaxf(mx0, __shfl_xor(mx0, 32)); mx1 = fmaxf(mx1, __shfl_xor(mx1, 32));
    const float cs = 0.08838834764831845f * 1.44269504089f;
    const u32x4 ones = {0x3f803f80u, 0x3f803f80u, 0x3f803f80u, 0x3f803f80u};
    const u32x2 ones2 = {0x3f803f80u, 0x3f803f80u};
    const __amdgpu_buffer_rsrc_t ro = rsrc(p.ws + (px ^ far_off(L_O + s * 262144 + hh * 8192)), 8192);
#pragma unroll
    for (int rt = 0; rt < 2; ++rt) {
      const float mc = (rt ? mx1 : mx0) * cs;
#define EX(v) __builtin_amdgcn_exp2f(__builtin_fmaf(v, cs, -mc))
      const u32x2 pm = {rt ? pk2(EX(m1[0]), EX(m1[1])) : pk2(EX(m0[0]), EX(m0[1])), 0u};
      const u32x4 px = rt ? (u32x4){pk2(EX(sb0[0]), EX(sb0[1])), pk2(EX(sb0[2]), EX(sb0[3])), pk2(EX(sb1[0]), EX(sb1[1])), pk2(EX(sb1[2]), EX(sb1[3]))}
                          : (u32x4){pk2(EX(sa[0]), EX(sa[1])), pk2(EX(sa[2]), EX(sa[3])), 0u, 0u};
      f32x4 o[8], sum;
      asm volatile("s_nop 1\n\t"
                   "v_mfma_f32_16x16x32_bf16 %0, %5, %15, 0\n\t"
                   "v_mfma_f32_16x16x32_bf16 %1, %7, %15, 0\n\tv_mfma_f32_16x16x32_bf16 %2, %8, %15, 0\n\t"
                   "v_mfma_f32_16x16x32_bf16 %3, %9, %15, 0\n\tv_mfma_f32_16x16x32_bf16 %4, %10, %15, 0\n\t"
                   "v_mfma_f32_16x16x16_bf16 %0, %6, %16, %0\n\t"
                   "v_mfma_f32_16x16x16_bf16 %1, %11, %16, %1\n\tv_mfma_f32_16x16x16_bf16 %2, %12, %16, %2\n\t"
                   "v_mfma_f32_16x16x16_bf16 %3, %13, %16, %3\n\tv_mfma_f32_16x16x16_bf16 %4, %14, %16, %4\n\ts_nop 7"
                   : "=&v"(sum), "=&v"(o[0]), "=&v"(o[1]), "=&v"(o[2]), "=&v"(o[3])
                   : "v"(ones), "v"(ones2), "v"(vx[0]), "v"(vx[1]), "v"(vx[2]), "v"(vx[3]), "v"(vm[0]), "v"(vm[1]), "v"(vm[2]), "v"(vm[3]), "v"(px), "v"(pm));
      asm volatile("s_nop 1\n\t"
                   "v_mfma_f32_16x16x32_bf16 %0, %4, %12, 0\n\tv_mfma_f32_16x16x32_bf16 %1, %5, %12, 0\n\t"
                   "v_mfma_f32_16x16x32_bf16 %2, %6, %12, 0\n\tv_mfma_f32_16x16x32_bf16 %3, %7, %12, 0\n\t"
                   "v_mfma_f32_16x16x16_bf16 %0, %8, %13, %0\n\tv_mfma_f32_16x16x16_bf16 %1, %9, %13, %1\n\t"
                   "v_mfma_f32_16x16x16_bf16 %2, %10, %13, %2\n\tv_mfma_f32_16x16x16_bf16 %3, %11, %13, %3\n\ts_nop 7"
                   : "=&v"(o[4]), "=&v"(o[5]), "=&v"(o[6]), "=&v"(o[7])
                   : "v"(vx[4]), "v"(vx[5]), "v"(vx[6]), "v"(vx[7]), "v"(vm[4]), "v"(vm[5]), "v"(vm[6]), "v"(vm[7]), "v"(px), "v"(pm));
      const float inv = __builtin_amdgcn_rcpf(sum[0]);
#pragma unroll
      for (int k = 0; k < 4; ++k) {
        const f32x4 a = o[2 * k] * inv, b = o[2 * k + 1] * inv;
        __builtin_amdgcn_raw_buffer_store_b128((u32x4){pk2(a[0], a[1]), pk2(a[2], a[3]), pk2(b[0], b[1]), pk2(b[2], b[3])}, ro, l * 16 + k * 2048 + rt * 1024, 0, 16);
      }
    }
    drain();
    publish(flags, myline, s + 1, l);
  }
}

DI unsigned role_r2(const Params& p, unsigned char* lds, int idx, const Boot& bt, unsigned px, int w, int l WACC) {
  unsigned* flags = bt.flags;
  const int g = l >> 4, q = (l >> 2) & 3, pp = l & 3;
  u32x32 wa0, wa1, wa2, wa3, wa4, wa5, wa6;
  {
    const uint16_t* wsrc = p.w2 + (size_t)w * 448 * 640 + idx * 64;
    const unsigned lw = w * 40960u, sw = (2 * g + (q >> 1)) & 3, tb = lw + (4 * g + q) * 128 + pp * 8;
    asm volatile(R2_LOAD_ASM
                 : WREG_OUT7
                 : [wlo] "s"((unsigned)(uint64_t)wsrc), [whi] "s"((unsigned)((uint64_t)wsrc >> 32)), [rs] "s"(1280u), [lw] "s"(lw),
                   [rmax] "s"(1720u - w * 448u), [vs] "v"((l >> 3) * 1280 + ((l & 7) ^ (((l >> 4) & 3) << 1)) * 16),
                   [t0] "v"(tb + (0 ^ sw) * 32), [t1] "v"(tb + (1 ^ sw) * 32), [t2] "v"(tb + (2 ^ sw) * 32), [t3] "v"(tb + (3 ^ sw) * 32)
                 : "memory", "scc", "s90", "s91", "s92", "s93");
  }
  if (px == ~1u) {
    px = wait_boot(bt, l, boot_look(bt, l));
    if (px & BOOT_BAD) return px;
  }
  GAS unsigned* ctr = (GAS unsigned*)(flags + (FL_UC + idx) * 32);
  volatile unsigned* shu = (volatile unsigned*)(lds + 131072);
  wg_barrier();
  if (w == 0 && l == 0) { shu[0] = __hip_atomic_fetch_add(ctr, 1u, RLX_AGENT); shu[1] = __hip_atomic_fetch_add(ctr, 1u, RLX_AGENT); }
  wg_barrier();
  int u = rfl(shu[0]), un = rfl(shu[1]);
  if (u >= 32) return px;
  unsigned last = 0;
  unsigned* done = flags + (FL_PG + idx * 4 + w) * 32;
  unsigned* prev = flags + FL_NUL * 32;
  LOOPSTART
  u32x32 r0, r1, r2; u32x16 r3;
#define R2_PRE(U)                                                                                                              \
  wait_ge(flags, FL_PH + ((l >> 3) & 1) * 584 + 7 * w + (l & 7) - ((l & 7) == 7), 2 * (U) + 1 + ((l >> 3) & 1), last WARG);    \
  asm volatile(R2_PRE_ASM : "={v[128:159]}"(r0), "={v[160:191]}"(r1), "={v[192:223]}"(r2), "={v[224:239]}"(r3)                \
               : [l16] "v"(l * 16), [src] "s"(p.ws + su(px ^ far_off(L_H + (U) * 262144 + w * 32768 + 4096))) : "memory", "scc", "s90", "s91")
  R2_PRE(u);
  for (int it = 0;; ++it) {
    f32x4 c[16];
    unsigned claim = 0;
    if (w == 0 && l == 0) claim = __hip_atomic_fetch_add(ctr, 1u, RLX_AGENT);
    asm volatile(R2_RUN_ASM
                 : [c0] "=&v"(c[0]), [c1] "=&v"(c[1]), [c2] "=&v"(c[2]), [c3] "=&v"(c[3]), [c4] "=&v"(c[4]), [c5] "=&v"(c[5]), [c6] "=&v"(c[6]), [c7] "=&v"(c[7]),
                   [c8] "=&v"(c[8]), [c9] "=&v"(c[9]), [c10] "=&v"(c[10]), [c11] "=&v"(c[11]), [c12] "=&v"(c[12]), [c13] "=&v"(c[13]), [c14] "=&v"(c[14]), [c15] "=&v"(c[15]),
                   "+{v[128:159]}"(r0), "+{v[160:191]}"(r1), "+{v[192:223]}"(r2), "+{v[224:239]}"(r3)
                 : [l16] "v"(l * 16), [src2] "s"(p.ws + su(px ^ far_off(L_H + u * 262144 + 131072 + w * 32768 + 4096))), [fl] "s"(prev), WREG_IN7
                 : "memory", "scc", "s90", "s91", "v240");
    if (w == 0 && l == 0) shu[2 + (it & 1)] = claim;
    R2_PRE(un < 32 ? un : u);
    f32x4* red = (f32x4*)(lds + (it & 1) * 65536);
#pragma unroll
    for (int t = 0; t < 16; ++t) red[(w * 16 + t) * 64 + l] = c[t];
    wg_barrier();
#pragma unroll
    for (int k = 0; k < 4; ++k) {
      const int t = w + 4 * k;
      const f32x4 y = (red[t * 64 + l] + red[(16 + t) * 64 + l]) + (red[(32 + t) * 64 + l] + red[(48 + t) * 64 + l]);
      float gv[4];
#pragma unroll
      for (int r = 0; r < 4; ++r) { const float yb = rbf(y[r]); gv[r] = yb * __builtin_amdgcn_rcpf(1.f + __builtin_amdgcn_exp2f(yb * -1.44269504089f)); }
      __builtin_amdgcn_raw_buffer_store_b64((u32x2){pk2(gv[0], gv[1]), pk2(gv[2], gv[3])}, rsrc(p.ws + (px ^ far_off(L_G + (2 * u + (k >> 1)) * 65536 + (k & 1) * 24576 + (idx * 4 + w) * 512)), 512), l * 8, 0, 16);
    }
    prev = done + u;
    if (un >= 32) break;
    u = un; un = rfl(shu[2 + (it & 1)]);
  }
  asm volatile("s_waitcnt vmcnt(0)" : "+{v[128:159]}"(r0), "+{v[160:191]}"(r1), "+{v[192:223]}"(r2), "+{v[224:239]}"(r3) : : "memory");
  publish(prev, 0, 1, l);
  return px;
}

DI void role_r3(const Params& p, unsigned char* lds, int cg, int grp, int late, unsigned* flags, unsigned px, int w, int l WACC) {
  const int g = l >> 4, q = (l >> 2) & 3, pp = l & 3;
  WREG_DECL
  {
    const uint16_t* wsrc = p.wo + (size_t)w * 1024 * 640 + cg * 32;
    const unsigned lw = w * 40960u, sw = g & 1, tb = lw + (4 * g + q) * 64 + pp * 8;
    asm volatile(R3_LOAD_ASM
                 : WREG_OUT
                 : [wlo] "s"((unsigned)(uint64_t)wsrc), [whi] "s"((unsigned)((uint64_t)wsrc >> 32)), [rs] "s"(1280u), [lw] "s"(lw), [rmax] "s"(4096u),
                   [vs] "v"((l >> 2) * 1280 + ((l & 3) ^ (((l >> 4) & 1) << 1)) * 16), [t0] "v"(tb + (0 ^ sw) * 32), [t1] "v"(tb + (1 ^ sw) * 32)
                 : "memory", "scc", "s90", "s91", "s92", "s93");
  }
  wg_barrier();
  u32x2* stash = (u32x2*)(lds + 32768) + w * 64 + l;
  const int xoff = ((16 * (w >> 1) + (l & 15)) * 640 + cg * 32 + 16 * (w & 1) + 4 * g) * 2;
  const __amdgpu_buffer_rsrc_t rx = rsrc(p.x, 2048u * 1280u), ro = rsrc(p.out, 2048u * 1280u);
#define GATE(S) __builtin_amdgcn_raw_buffer_load_b64(rsrc(p.ws + (px ^ far_off(L_G + (S) * 65536 + (w >> 1) * 24576 + (cg * 2 + (w & 1)) * 512)), 512), l * 8, 0, 16)
#define XROW(S) __builtin_amdgcn_raw_buffer_load_b64(rx, xoff + (S) * 40960, 0, 0)
#define FINISH(S, XV, GV)                                                                                                          \
  {                                                                                                                                \
    const u32x2 y = stash[((S) >> 1) * 256];                                                                                       \
    const unsigned a0 = pk2(bflo(y[0]) * bflo(GV[0]), bfhi(y[0]) * bfhi(GV[0])), a1 = pk2(bflo(y[1]) * bflo(GV[1]), bfhi(y[1]) * bfhi(GV[1])); \
    __builtin_amdgcn_raw_buffer_store_b64((u32x2){pk2(bflo(XV[0]) + bflo(a0), bfhi(XV[0]) + bfhi(a0)), pk2(bflo(XV[1]) + bflo(a1), bfhi(XV[1]) + bfhi(a1))}, \
                                          ro, xoff + (S) * 40960, 0, 0);                                                           \
  }
  int fin = late ? 64 : grp;
  LOOPSTART
  for (int s = fin; s < 62; s += 2) {
    unsigned seen = 0;
    wait_ge(flags + (l < 32 ? 0 : l - 32), l < 32 ? FL_PO + grp * 64 + w * 8 + (l & 7) : FL_PG + cg * 2 + (w & 1), l < 32 ? s + 1 : 0, seen WARG);
    const unsigned gm = (unsigned)(__ballot(seen != 0) >> 32);
    const int t1 = min(fin + 2, s);
    const bool ok0 = (gm >> (fin >> 1)) & 1, ok1 = ok0 && fin < s && ((gm >> (t1 >> 1)) & 1);
    const u32x2 x0 = XROW(fin), g0 = GATE(fin), x1 = XROW(t1), g1 = GATE(t1);
    f32x4 c0, c1, c2, c3;
    asm volatile(R3_MAIN_ASM
                 : [c0] "=&v"(c0), [c1] "=&v"(c1), [c2] "=&v"(c2), [c3] "=&v"(c3)
                 : [l16] "v"(l * 16), [src] "s"(p.ws + su(px ^ far_off(L_O + s * 262144 + w * 65536 + 4096))), WREG_IN
                 : "memory", "scc", R3_MAIN_CLOB);
    f32x4* red = (f32x4*)(lds + (s & 2) * 8192);
    red[(w * 4 + 0) * 64 + l] = c0; red[(w * 4 + 1) * 64 + l] = c1; red[(w * 4 + 2) * 64 + l] = c2; red[(w * 4 + 3) * 64 + l] = c3;
    wg_barrier();
    const f32x4 y = (red[w * 64 + l] + red[(4 + w) * 64 + l]) + (red[(8 + w) * 64 + l] + red[(12 + w) * 64 + l]);
    stash[(s >> 1) * 256] = (u32x2){pk2(y[0], y[1]), pk2(y[2], y[3])};
    if (ok0) { FINISH(fin, x0, g0) fin += 2; }
    if (ok1) { FINISH(fin, x1, g1) fin += 2; }
  }
  for (; fin < 62; fin += 2) {
    unsigned seen = 0;
    wait_ge(flags + (fin >> 1), FL_PG + cg * 2 + (w & 1), 1, seen WARG);
    const u32x2 x0 = XROW(fin), g0 = GATE(fin);
    FINISH(fin, x0, g0)
  }
  {
    const int s = 62 + grp, nt = w & 1;
    unsigned seen = 0;
    wait_ge(flags + 31, FL_PG + cg * 2 + nt, 1, seen WARG);
    const int xo = ((s * 32 + 16 * late + (l & 15)) * 640 + cg * 32 + 16 * nt + 4 * g) * 2;
    const u32x2 xv = __builtin_amdgcn_raw_buffer_load_b64(rx, xo, 0, 0);
    const u32x2 gv = __builtin_amdgcn_raw_buffer_load_b64(rsrc(p.ws + (px ^ far_off(L_G + s * 65536 + late * 24576 + (cg * 2 + nt) * 512)), 512), l * 8, 0, 16);
    seen = 0;
    wait_ge(flags, FL_PO + grp * 64 + w * 8 + (l & 7), s + 1, seen WARG);
    f32x4 c0, c1;
    asm volatile(R3_HALF_ASM
                 : [c0] "=&v"(c0), [c1] "=&v"(c1)
                 : [l16] "v"(l * 16), [src] "s"(p.ws + su(px ^ far_off(L_O + s * 262144 + w * 65536 + late * 1024 + 4096))), WREG_IN
                 : "memory", "scc", R3_HALF_CLOB);
    f32x4* red = (f32x4*)(lds + 98304);
    red[(w * 2 + 0) * 64 + l] = c0; red[(w * 2 + 1) * 64 + l] = c1;
    wg_barrier();
    if (w < 2) {
      const f32x4 y = (red[w * 64 + l] + red[(2 + w) * 64 + l]) + (red[(4 + w) * 64 + l] + red[(6 + w) * 64 + l]);
      const unsigned y0 = pk2(y[0], y[1]), y1 = pk2(y[2], y[3]);
      const unsigned a0 = pk2(bflo(y0) * bflo(gv[0]), bfhi(y0) * bfhi(gv[0])), a1 = pk2(bflo(y1) * bflo(gv[1]), bfhi(y1) * bfhi(gv[1]));
      __builtin_amdgcn_raw_buffer_store_b64((u32x2){pk2(bflo(xv[0]) + bflo(a0), bfhi(xv[0]) + bfhi(a0)), pk2(bflo(xv[1]) + bflo(a1), bfhi(xv[1]) + bfhi(a1))}, ro, xo, 0, 0);
    }
  }
}

#define MARG
#define MVAL
#define MVAL0
#define DEVEND
#define DONE DEVEND __builtin_amdgcn_endpgm();
template <int att>
DI unsigned run_roles(unsigned char* lds, int w0 MARG) {
  unsigned* sh = (unsigned*)lds;
  const __attribute__((address_space(4))) Params* ka = (const __attribute__((address_space(4))) Params*)__builtin_amdgcn_kernarg_segment_ptr();
  Params p;
#define LOAD_ARGS                                                                                                                   \
  asm volatile("" : "+s"(ka));                                                                                                      \
  p.x = ka->x; p.mem0 = ka->mem0; p.wq = ka->wq; p.wk = ka->wk; p.wv = ka->wv; p.wo = ka->wo; p.w1 = ka->w1; p.b1 = ka->b1;         \
  p.w2 = ka->w2; p.out = ka->out; p.tw = ka->tw; p.tr = ka->tr;                                                                    \
  p.ws = ka->ws + ((0 - (uintptr_t)ka->ws) & ((4u << 20) - 1));                                                                    \
  asm volatile("" : "+s"(p.ws));
  asm volatile("" : "+s"(ka));
  p.ws = ka->ws + ((0 - (uintptr_t)ka->ws) & ((4u << 20) - 1));
  unsigned* flags = (unsigned*)(p.ws + OFF_FLAGS + att * (512u << 10));
  GAS unsigned long long* boot = (GAS unsigned long long*)(p.ws + OFF_BOOT);
  int bid = blockIdx.x, w = w0;
  asm volatile("" : "+s"(bid), "+s"(w));
  int l;
  asm volatile("v_mbcnt_lo_u32_b32 %0, -1, 0\n\tv_mbcnt_hi_u32_b32 %0, -1, %0" : "=v"(l));
  const int tid = w * 64 + l;
  const unsigned xcc = __builtin_amdgcn_s_getreg(20 | (3 << 11)) & 7u;
  const unsigned grp8 = att ? (bid & 7u) : xcc;
  Boot bt;
  bt.words = p.ws + OFF_BOOT + (256 + att * 256) * 128;
  bt.salt = ((grp8 - bid) & 7u) << 2;
  bt.mine = bt.words + bid * 128;
  bt.epoch = p.ws + OFF_BOOT;
  bt.flags = flags;
#define SALT(E) bt.salt |= (((unsigned long long)rfl((unsigned)((E) >> 32)) << 32 | rfl((unsigned)(E))) + 32) & ~31ull
  const int xg = grp8 & 3, rk = bid >> 3;
  const int role = grp8 < 4 ? (rk < 16 ? xg * 16 + rk : 64 + xg * 16 + (rk - 16)) : (int)grp8 * 32 + rk;
  const __amdgpu_buffer_rsrc_t rboot = rsrc(bt.words + bid * 128, 128);
#define ARRIVE(V) __builtin_amdgcn_raw_buffer_store_b128((u32x4){(unsigned)bt.salt | (V), (unsigned)(bt.salt >> 32), 0u, 0u}, rboot, l * 16, 0, 16)
  unsigned px = ~0u;
  unsigned char* MA = p.ws + (2u << 20) + 8192;
  unsigned char* MB = p.ws + 80 * 16384;
  unsigned char* TL = p.ws + 96 * 16384;
  if (role < 64) {
    LOAD_ARGS
    const int h = role >> 1, half = role & 1;
    const int c = l & 15, g = l >> 4, q = (l >> 2) & 3, pp = l & 3;
    const unsigned qsrc = (16 * w + g) * 8192 + ((c ^ ((g | ((w & 1) << 2)) << 1)) * 16);
    const unsigned vsrc = (16 * w + (l >> 3)) * 8192 + (((l & 7) ^ ((((l >> 4) & 1) | ((w & 1) << 1)) << 1)) * 16);
    u32x2 ev;
    asm volatile(WORKER_EARLY_ASM
                 : "=&{v[108:109]}"(ev)
                 : [wave] "s"(w), [wq] "s"(p.wq + h * 128), [wk] "s"(p.wk + h * 128), [wv] "s"(p.wv + h * 128 + half * 64),
                   [wo] "s"(p.wo + (size_t)(h * 128 + half * 64) * 640), [ep] "s"(bt.epoch), [l16] "v"(l * 16), [qs] "v"(qsrc), [vs] "v"(vsrc)
                 : "memory", "scc", "s20", "s21", "s22", "s23", "s24", "s25", "s26", "s27", "s36", "s62", "s63", "s66", "s67");
    sh[0 * 256 + tid] = (c & 1) ? 0xEEEEu : 0x4444u;
    sh[1 * 256 + tid] = l * 16;
    sh[4 * 256 + tid] = ((c >> 2) * 2 + (g & 1)) * 128 + (((g >> 1) ^ (c >> 3)) * 4 + (c & 3)) * 16;
    sh[5 * 256 + tid] = (2 * w * 8 + g * 2 + (c >> 3)) * 16384 + (c & 7) * 16;
    sh[6 * 256 + tid] = (64 + (w >> 1) * 8 + g * 2 + (w & 1)) * 16384 + c * 8;
    sh[7 * 256 + tid] = 10240 + (32 * w + c) * 32 + (g ^ (c >> 2)) * 8;
    sh[8 * 256 + tid] = 10240 + (l >> 2) * 32 + (pp ^ g) * 8;
    sh[9 * 256 + tid] = (c & 2) ? 14336 + (2 * (c >> 2) + (c & 1)) * 144 + (16 * w + 4 * g) * 2 : 15488 + l * 8;
    sh[10 * 256 + tid] = 14336 + (c >> 1) * 144 + g * 32 + (c & 1) * 16;
    sh[11 * 256 + tid] = 16384 + w * 30720 + l * 16;
    sh[12 * 256 + tid] = qsrc;
    sh[13 * 256 + tid] = vsrc;
    {
      const int rl = 16 * (q & 1) + 2 * (q >> 1) + 8 * (g >> 1) + (g & 1), f = (rl & 3) | (((rl >> 4) & 1) << 2), gv = ((rl >> 1) & 1) | (((rl >> 4) & 1) << 1);
      sh[14 * 256 + tid] = rl * 256 + (((2 * w) ^ f) * 32) + pp * 8; sh[15 * 256 + tid] = rl * 256 + (((2 * w + 1) ^ f) * 32) + pp * 8;
      sh[18 * 256 + tid] = rl * 128 + ((w ^ gv) * 32) + pp * 8;
    }
    {
      const int rl = 32 * (g >> 1) + 16 * (q & 1) + 4 * (q >> 1) + 2 * (g & 1), f = (rl & 3) | (((rl >> 4) & 1) << 2), gv = ((rl >> 1) & 1) | (((rl >> 4) & 1) << 1);
      sh[16 * 256 + tid] = rl * 256 + (((2 * w) ^ f) * 32) + pp * 8; sh[17 * 256 + tid] = rl * 256 + (((2 * w + 1) ^ f) * 32) + pp * 8;
      sh[19 * 256 + tid] = rl * 128 + ((w ^ gv) * 32) + pp * 8;
    }
    sh[20 * 256 + tid] = (16 * (g >> 1) + 8 * (q >> 1) + 4 * (g & 1) + (q & 1)) * 1280 + (160 * w + 4 * pp) * 2;
    if (l == 0) {
      uint64_t* sc = (uint64_t*)(lds + 24576 + w * 256);
      const unsigned fk = (FL_PK + (h >> 1) * 4 + 2 * (h & 1)) * 128, fv = (FL_PV + (h >> 1) * 4 + 2 * (h & 1) + half) * 128;
      sc[0] = (uint64_t)(p.wq + h * 128); sc[1] = (uint64_t)(p.wk + h * 128); sc[2] = (uint64_t)(p.wv + h * 128 + half * 64);
      sc[3] = (uint64_t)(p.wo + (size_t)(h * 128 + half * 64) * 640);
      sc[4] = (uint64_t)(TL + 16384 * w + role * 128); sc[5] = (uint64_t)TL; sc[6] = (uint64_t)flags;
      sc[7] = (uint64_t)(unsigned)p.tw | ((uint64_t)fk << 32);
      sc[8] = (uint64_t)(p.ws + OFF_DBG + 24576 + (role * 4 + w) * 16); sc[9] = (uint64_t)(p.ws + OFF_DBG + 65536 + (role * 4 + w) * 1024);
      sc[10] = (uint64_t)(p.ws + OFF_DBG + 524288 + (role * 4 + w) * 4096);
      sc[11] = (uint64_t)(MA + w * 2048); sc[12] = (uint64_t)(MB + w * 512); sc[13] = (uint64_t)(p.ws + role * 128);
      sc[14] = (uint64_t)(p.ws + far_off(L_KX + h * 8192 + w * 2048)); sc[15] = (uint64_t)(p.ws + far_off(L_VX + h * 8192 + (4 * half + w) * 1024));
      sc[16] = (uint64_t)(p.ws + near_off(L_KM + h * 4096 + w * 1024 + half * 512)); sc[17] = (uint64_t)(p.ws + near_off(L_VM + h * 2048 + (4 * half + w) * 256));
      sc[18] = (uint64_t)(unsigned)(w * 1024 + half * 512) | ((uint64_t)(fk + 128) << 32);
      sc[19] = (uint64_t)flags; sc[20] = (uint64_t)(flags + (FL_PM + role * 4 + w) * 32); sc[21] = fv; sc[22] = (uint64_t)bt.words; sc[23] = bt.salt; sc[24] = (uint64_t)bt.mine; sc[25] = (uint64_t)(p.ws + 100 * 16384 + w * 512);
    }
    asm volatile("s_waitcnt lgkmcnt(0)" ::: "memory");
    asm volatile(WORKER_ASM : "+{v[108:109]}"(ev) : [wave] "s"(w) : "memory", "vcc", "scc", WORKER_CLOB);
    px = BOOT_BAD;
  } else if (role < NCHAIN) {
    LOAD_ARGS
    const int rc = role - 64;
    const int rq = (rc & 15) * 4 + (rc >> 4);
    const int r0 = rq < 16 ? (rq >> 2) * 16 + (rq & 3) * 2 : (rq < 48 ? ((rq - 16) >> 3) * 16 + 8 + (rq & 7) : 16 + rq);
    const unsigned long long e0 = __hip_atomic_load(boot, RLX_AGENT);
    layernorm_rows(p, rq * 32 + w * 8, l);
    if (tid < 152) __builtin_amdgcn_raw_buffer_store_b128((u32x4){0u, 0u, 0u, 0u}, rsrc(p.ws, 8u << 20), tid < 136 ? OFF_FLAGS + att * (512u << 10) + rq * 2176 + tid * 16 : 96 * 16384 + rq * 256 + (tid - 136) * 16, 0, 16);
    if (tid >= 160 && tid < (rq < 16 ? 176 : 168)) init_m0(p, r0 + ((tid >> 3) & 1), tid & 7);
    if (tid == 192 && rq < 8) sh[0] = 0;
    if (tid == 192 && rq >= 8) {
      GAS unsigned* a = (GAS unsigned*)(p.ws + (112 + (rq & 15)) * 16384 + (rq >> 4) * 2048);
      int score = 0;
#pragma unroll 1
      for (int i = 0; i < 4; ++i) {
        unsigned long long t1, t2;
        asm volatile("global_store_dword %2, %4, off sc1\n\tglobal_store_dword %3, %4, off sc1\n\ts_waitcnt vmcnt(1)\n\ts_memtime %0\n\ts_waitcnt vmcnt(0)\n\ts_memtime %1\n\ts_waitcnt lgkmcnt(0)"
                     : "=&s"(t1), "=&s"(t2) : "v"(a + (i & 1) * 2048), "v"(a + ((i & 1) ^ 1) * 2048), "v"(0u) : "memory");
        if (i >= 2) score += (i & 1) ? -(int)(t2 - t1) : (int)(t2 - t1);
      }
      sh[0] = score <= 0;
    }
    drain();
    SALT(e0);
    wg_barrier();
    if (w == 3) ARRIVE(sh[0]);
    else if (w) {
      TOUCH_DELAY
      const int t = (rc & 15) * 128 + tid - 64;
      unsigned acc = 0;
#pragma unroll
      for (int k = TOUCH_FROM; k < 5; ++k) {
        const int i = t + 2048 * k, off = (i >> 4) * 4096 + xg * 1024 + (i & 15) * 64;
        acc += p.wq[off] + p.wk[off] + p.wv[off];
      }
      asm volatile("" ::"v"(acc));
    }
    px = wait_boot(bt, l, boot_look(bt, l));
    if (!(px & BOOT_BAD)) {
    if (w == 0) {
      init_line(p, r0, px, l);
      if (rq < 16) init_line(p, r0 + 1, px, l);
      drain();
      publish(flags, FL_RDY + rq, 1, l);
      if (rc == 0 && l == 0) __hip_atomic_store(boot, bt.salt & ~31ull, RLX_AGENT);
    }
    int r = (w == 0 || (w == 1 && rq < 16)) ? r0 + w : -1;
    r = rfl(r);
    if (r >= 0) {
      const int c = l & 15, g = l >> 4;
      bf16x8 sel;
      const int pj = c < 4 ? 2 * c : (c < 8 ? 2 * (c - 4) + 1 : -1);
#pragma unroll
      for (int j = 0; j < 8; ++j) sel[j] = (__bf16)(j == pj ? 1.0f : 0.0f);
      const int oaddr = (((((c & 7) >> 2) ^ ((r >> 2) & 1)) * 4) + (g & 1) * 2 + ((c >> 1) & 1)) * 16 + (c & 1) * 8;
      const int isb = r >> 6;
      unsigned char* mo = p.ws + (((isb ? 80 * 16384 - 8192 : (2 << 20) + 8192) + r * 128) ^ px);
      asm volatile(REDUCER_ASM
                   :
                   : [l16] "v"(l * 16), [sel] "v"(sel), [oaddr] "v"(oaddr), [fl] "v"(l * 128), [pl] "s"(p.ws + r * 16384 + 4096 + px), [pl2] "s"(p.ws + ((unsigned)OFF_P2 ^ px) + r * 16384 + 4096), [mo] "s"(mo), [isb] "s"(isb),
                     [err] "s"(flags), [tli] "s"(TL + px), [tw] "s"(p.tw + 40), [tr] "s"((p.tr >> (isb ? 0 : ((r >> 3) & 1) ? 8 : 16)) & 255),
                     [dbg] "s"(p.ws + OFF_DBG + 16384 + r * 16), [trc] "s"(p.ws + OFF_DBG + 65536 + (256 + r) * 1024)
                   : "memory", "vcc", "scc", REDUCER_CLOB);
    }
    drain();
    __builtin_amdgcn_endpgm();
    }
    drain();
  } else {
    const int nf2 = xg < 2 ? 3 : 2;
    const int e = rk - 26 < nf2 ? 14 + (xg < 2 ? 3 * xg : 2 + 2 * xg) + rk - 26 : (xg < 2 ? 3 * xg : 4 * xg - 2) + rk - 26 - nf2;
    const bool hlp = rk >= 12 && rk < 28;
    LOAD_ARGS
    if (rk < 28) {
      const int idx = xg * 4 + (rk & 3), grp = hlp ? 1 + ((rk - 12) >> 3) : 0;
      if (hlp ? (rk & 4) == 0 : (rk & 12) == 4) px = role_r1<KIND_V>(p, lds, idx, grp, hlp, false, bt, px, w, l);
      else px = role_r1<KIND_K>(p, lds, idx, grp, hlp, (rk & 28) == 8, bt, px, w, l);
    } else if (e < 14) {
      px = role_r1<KIND_F>(p, lds, e >> 1, e & 1, 0, false, bt, px, w, l);
    } else {
      const unsigned long long e0 = __hip_atomic_load(boot, RLX_AGENT);
      SALT(e0);
      wg_barrier();
      if (w == 3) ARRIVE(0u);
      px = ~1u;
    }
    if (!(px & BOOT_BAD)) {
      LOAD_ARGS
      if (rk >= 26) px = role_r2(p, lds, e < 10 ? e : e < 14 ? e - 10 : e - 14, bt, px, w, l WARG);
      else if (rk >= 12 && rk < 16) { role_at(p, xg * 2 + (rk & 1), (rk >> 1) & 1, flags, px, w, l WARG); DONE }
      else if (rk < 10 || rk >= 16) {
        const int j = rk < 10 ? rk : rk - 16;
        if (rk < 10) { DEVEND }
        role_r3(p, lds, xg * 5 + j % 5, j / 5, rk < 10, flags, px, w, l WARG);
        if (rk >= 16) { DEVEND }
        __builtin_amdgcn_endpgm();
      }
    }
    DEVEND
  }
  return px;
}

__global__ void __launch_bounds__(256, 1) main_kernel(Params p0) {
  extern __shared__ __attribute__((aligned(16))) unsigned char lds[];
  const int w0 = rfl(threadIdx.x >> 6);
  if (run_roles<0>(lds, w0 MVAL) & BOOT_BAD) {
    wg_barrier();
    run_roles<1>(lds, w0 MVAL0);
  }
}

static void launch(void* const* d_in, void* d_out, void* d_ws, int tw, int tr, hipStream_t stream) {
  Params p;
  p.x = (const uint16_t*)d_in[0]; p.mem0 = (const uint16_t*)d_in[1]; p.wq = (const uint16_t*)d_in[2]; p.wk = (const uint16_t*)d_in[3];
  p.wv = (const uint16_t*)d_in[4]; p.wo = (const uint16_t*)d_in[5]; p.w1 = (const uint16_t*)d_in[8]; p.b1 = (const uint16_t*)d_in[9];
  p.w2 = (const uint16_t*)d_in[10]; p.out = (uint16_t*)d_out;
  p.ws = (unsigned char*)d_ws;
  p.tw = tw; p.tr = tr;
  (void)hipFuncSetAttribute((const void*)main_kernel, hipFuncAttributeMaxDynamicSharedMemorySize, LDS_BYTES);
  hipLaunchKernelGGL(main_kernel, dim3(256), dim3(256), LDS_BYTES, stream, p);
}

extern "C" void kernel_launch(void* const* d_in, const int* in_sizes, int n_in, void* d_out, int out_size, void* d_ws, size_t ws_size, hipStream_t stream) {
  launch(d_in, d_out, d_ws, T_WORK, T_RED | (T_RED << 8) | ((T_RED - T_EARLY) << 16), stream);
}
```
